```python
import torch

def _softmax(x, axis=-1):
    return torch.softmax(x, dim=axis)
def _gelu(x):
    return torch.nn.functional.gelu(x, approximate="tanh")
def _silu(x):
    return torch.nn.functional.silu(x)
def _sigmoid(x):
    return torch.sigmoid(x)
def _layernorm(x, axis=-1, eps=1e-5):
    m = x.mean(dim=axis, keepdim=True)
    v = x.var(dim=axis, keepdim=True, unbiased=False)
    return (x - m) / torch.sqrt(v + eps)
def _scatter_add(tgt, idx, src, axis=0):
    return tgt.clone().index_add_(axis, idx, src)
def _segment_sum(src, seg, n):
    out = torch.zeros((n,) + src.shape[1:], dtype=src.dtype)
    return out.index_add_(0, seg, src)
def _pad(x, pw):
    flat = tuple(v for pair in reversed(pw) for v in pair)
    return torch.nn.functional.pad(x, flat)

kernel_name = 'sequential_preLN__gqa_block_cached__mixer_token_mlp__decode'

def setup_inputs():

    _w = (lambda a: (lambda t: t.to(torch.bfloat16) if t.is_floating_point() else t)(torch.as_tensor(a)))
    def _A_setup():
        B, Sq, D = 8, 1, 512
        Hq, Hkv, Sk, Dh = 32, 8, 8192, 128
        assert Hq % Hkv == 0, (Hq, Hkv)
        return {
            "x": _w((lambda *s: torch.randn(*s, dtype=torch.bfloat16) * 0.05)(B, Sq, D)),
            "k_cache": _w((lambda *s: torch.randn(*s, dtype=torch.bfloat16) * 1.6)(B, Hkv, Sk, Dh)),
            "v_cache": _w((lambda *s: torch.randn(*s, dtype=torch.bfloat16) * 0.8)(B, Hkv, Sk, Dh)),
            "wq": _w((lambda *s: torch.randn(*s, dtype=torch.bfloat16) * 1.6)(D, Hq * Dh) * (D ** -0.5)),
            "wo": _w((lambda *s: torch.randn(*s, dtype=torch.bfloat16) * 0.8)(Hq * Dh, D) * ((Hq * Dh) ** -0.5)),
        }
    _A_in = _A_setup()

    def _B_setup():
        B, S, D, F = 8, 1, 512, 1792
        return {
            "x": _w((lambda *s: torch.randn(*s, dtype=torch.bfloat16) * 0.05)(B, S, D)),
            "w1": _w((lambda *s: torch.randn(*s, dtype=torch.bfloat16) * 1.6)(S, F) * (S ** -0.5)),
            "b1": _w((lambda *s: torch.randn(*s, dtype=torch.bfloat16) * 1.6)(F) * 0.02),
            "w2": _w((lambda *s: torch.randn(*s, dtype=torch.bfloat16) * 1.6)(F, S) * (F ** -0.5)),
            "b2": _w((lambda *s: torch.randn(*s, dtype=torch.bfloat16) * 0.8)(S) * 0.02),
        }
    _B_in = _B_setup()

    return {
        "A_x": _A_in["x"],
        "A_k_cache": _A_in["k_cache"],
        "A_v_cache": _A_in["v_cache"],
        "A_wq": _A_in["wq"],
        "A_wo": _A_in["wo"],
        "B_x": _B_in["x"],
        "B_w1": _B_in["w1"],
        "B_b1": _B_in["b1"],
        "B_w2": _B_in["w2"],
        "B_b2": _B_in["b2"],
    }

def reference(A_x, A_k_cache, A_v_cache, A_wq, A_wo, B_x, B_w1, B_b1, B_w2, B_b2):
    def _A_ref(x, k_cache, v_cache, wq, wo):
        B, Sq, D = x.shape
        Hq, Dh = 32, 128
        Hkv = k_cache.shape[1]
        rep = Hq // Hkv
        q = (lambda x, *d: x.permute(*d))((x @ wq).reshape(B, Sq, Hq, Dh), 0, 2, 1, 3)
        k = (lambda x, n, axis: torch.repeat_interleave(x, n, dim=axis))(k_cache, rep, axis=1)
        v = (lambda x, n, axis: torch.repeat_interleave(x, n, dim=axis))(v_cache, rep, axis=1)
        scores = torch.einsum("bhqd,bhkd->bhqk", q, k) * (Dh ** -0.5)
        p = _softmax(scores, axis=-1)
        o = torch.einsum("bhqk,bhkd->bhqd", p, v)
        return (lambda x, *d: x.permute(*d))(o, 0, 2, 1, 3).reshape(B, Sq, Hq * Dh) @ wo

    def _B_ref(x, w1, b1, w2, b2):
        B, S, D = x.shape
        h = _layernorm(x, axis=-1)
        u = torch.einsum("bsd,sf->bdf", h, w1) + b1
        u = _gelu(u)
        y = torch.einsum("bdf,fs->bds", u, w2) + b2
        return (lambda x, *d: x.permute(*d))(y, 0, 2, 1)

    x = A_x
    h = x + _A_ref(_layernorm(x, axis=-1), A_k_cache, A_v_cache, A_wq, A_wo)
    h2 = h + _B_ref(_layernorm(h, axis=-1), B_w1, B_b1, B_w2, B_b2)
    return h2
```

```cpp
#include <hip/hip_runtime.h>
#include <hip/hip_bf16.h>
#include <cstdint>

constexpr int B = 8, DM = 512, HQ = 32, HKV = 8, SK = 8192, DH = 128, G = 4, FF = 1792;
constexpr int NSPLIT = 8;
typedef unsigned u32x4 __attribute__((ext_vector_type(4)));
typedef float f32x4 __attribute__((ext_vector_type(4)));
using bf16x2 = __attribute__((ext_vector_type(2))) __bf16;

__device__ __forceinline__ float bf2f(unsigned short b) { return __builtin_bit_cast(float, (unsigned)b << 16); }
__device__ __forceinline__ unsigned short f2bf(float f) { return __builtin_bit_cast(unsigned short, (__bf16)f); }
__device__ __forceinline__ float dot2(unsigned k, unsigned q, float acc) {
  return __builtin_amdgcn_fdot2_f32_bf16(__builtin_bit_cast(bf16x2, k), __builtin_bit_cast(bf16x2, q), acc, false);
}
template <int CTRL> __device__ __forceinline__ float dpp(float x) {
  return __builtin_bit_cast(float, __builtin_amdgcn_mov_dpp(__builtin_bit_cast(int, x), CTRL, 0xf, 0xf, true));
}
constexpr int XOR1 = 0xB1, XOR2 = 0x4E, XOR7 = 0x141, XOR8 = 0x128, ROR4 = 0x124;
__device__ __forceinline__ float xrow16_max(float x) {
  auto s = __builtin_amdgcn_permlane16_swap(__float_as_uint(x), __float_as_uint(x), false, false);
  x = fmaxf(__uint_as_float(s[0]), __uint_as_float(s[1]));
  auto t = __builtin_amdgcn_permlane32_swap(__float_as_uint(x), __float_as_uint(x), false, false);
  return fmaxf(__uint_as_float(t[0]), __uint_as_float(t[1]));
}
__device__ __forceinline__ float xrow16_sum(float x) {
  auto s = __builtin_amdgcn_permlane16_swap(__float_as_uint(x), __float_as_uint(x), false, false);
  x = __uint_as_float(s[0]) + __uint_as_float(s[1]);
  auto t = __builtin_amdgcn_permlane32_swap(__float_as_uint(x), __float_as_uint(x), false, false);
  return __uint_as_float(t[0]) + __uint_as_float(t[1]);
}
__device__ __forceinline__ float wave_sum(float x) {
  for (int o = 32; o > 0; o >>= 1) x += __shfl_xor(x, o);
  return x;
}

__global__ void __launch_bounds__(512) k_q(const unsigned short* x, const unsigned short* wq, unsigned short* q) {
  __shared__ float xn[8][512];
  __shared__ float red[8][128];
  const int tid = threadIdx.x, lane = tid & 63, w = tid >> 6;
  {
    u32x4 v = *(const u32x4*)(x + w * 512 + lane * 8);
    float f[8];
    for (int k = 0; k < 4; ++k) { f[2 * k] = __builtin_bit_cast(float, v[k] << 16); f[2 * k + 1] = __builtin_bit_cast(float, v[k] & 0xffff0000u); }
    float s = 0; for (int k = 0; k < 8; ++k) s += f[k];
    s = wave_sum(s); float m = s * (1.f / 512);
    float ss = 0; for (int k = 0; k < 8; ++k) { float d = f[k] - m; ss += d * d; }
    ss = wave_sum(ss); float r = 1.f / sqrtf(ss * (1.f / 512) + 1e-5f);
    for (int k = 0; k < 8; ++k) xn[w][lane * 8 + k] = bf2f(f2bf((f[k] - m) * r));
  }
  __syncthreads();
  const int c0 = blockIdx.x * 16;
  u32x4 w0 = *(const u32x4*)(wq + (size_t)tid * 4096 + c0), w1 = *(const u32x4*)(wq + (size_t)tid * 4096 + c0 + 8);
  float wf[16];
  for (int k = 0; k < 4; ++k) { wf[2 * k] = __builtin_bit_cast(float, w0[k] << 16); wf[2 * k + 1] = __builtin_bit_cast(float, w0[k] & 0xffff0000u);
    wf[8 + 2 * k] = __builtin_bit_cast(float, w1[k] << 16); wf[8 + 2 * k + 1] = __builtin_bit_cast(float, w1[k] & 0xffff0000u); }
  for (int b = 0; b < 8; ++b) {
    float xv = xn[b][tid];
    for (int n = 0; n < 16; ++n) { float s = wave_sum(xv * wf[n]); if (lane == 0) red[w][b * 16 + n] = s; }
  }
  __syncthreads();
  if (tid < 128) { float s = 0; for (int ww = 0; ww < 8; ++ww) s += red[ww][tid]; q[(tid >> 4) * 4096 + c0 + (tid & 15)] = f2bf(s); }
}

constexpr int PAGE = 16, WAVES = 4;
constexpr int PPS = SK / PAGE / NSPLIT, PPW = PPS / WAVES;
constexpr int MRG_REC = 4 + DH;
__global__ void __launch_bounds__(256) k_attn(const unsigned short* q, const unsigned short* kc, const unsigned short* vc, float* pml, float* po) {
  __shared__ __attribute__((aligned(16))) float lds[WAVES * 64 + WAVES * 4 * MRG_REC];
  const int s = blockIdx.x, kvh = blockIdx.y, b = blockIdx.z;
  const int tid = threadIdx.x, lane = tid & 63, wave = __builtin_amdgcn_readfirstlane(tid >> 6), r = lane >> 4, c = lane & 15;
  constexpr float C2 = 0.088388347648318440f * 1.4426950408889634f;
  const int p0 = s * PPS;
  u32x4 qpk[4];
#pragma unroll
  for (int g = 0; g < 4; ++g) qpk[g] = *(const u32x4*)(q + (size_t)b * 4096 + (kvh * 4 + g) * DH + 8 * c);
  const size_t base = ((size_t)(b * HKV + kvh) * SK) * DH + (size_t)r * DH + 8 * c;
  u32x4 Kn[4], Vn[4];
#define ISSUE(i_) do { const size_t o_ = base + (size_t)(p0 + wave + 4 * (i_)) * PAGE * DH; \
    _Pragma("unroll") for (int t_ = 0; t_ < 4; ++t_) { Kn[t_] = __builtin_nontemporal_load((const u32x4*)(kc + o_ + t_ * 4 * DH)); Vn[t_] = __builtin_nontemporal_load((const u32x4*)(vc + o_ + t_ * 4 * DH)); } } while (0)
  float m = -INFINITY, l = 0.f, acc[32];
#pragma unroll
  for (int k = 0; k < 32; ++k) acc[k] = 0.f;
  ISSUE(0);
  for (int i = 0; i < PPW; ++i) {
    u32x4 Kc[4], Vc[4];
#pragma unroll
    for (int t = 0; t < 4; ++t) { Kc[t] = Kn[t]; Vc[t] = Vn[t]; }
    if (i + 1 < PPW) ISSUE(i + 1);
    float x[16];
#pragma unroll
    for (int t = 0; t < 4; ++t)
#pragma unroll
      for (int g = 0; g < 4; ++g) { float d = 0.f;
#pragma unroll
        for (int k = 0; k < 4; ++k) d = dot2(Kc[t][k], qpk[g][k], d);
        x[t * 4 + g] = d; }
#define TR_STEP(HALF, CTRL, BIT) _Pragma("unroll") for (int h = 0; h < (HALF); ++h) { \
      const float lo_ = x[h], hi_ = x[h + (HALF)]; const float t1_ = lo_ + dpp<CTRL>(lo_), t2_ = hi_ + dpp<CTRL>(hi_); \
      x[h] = (c & (BIT)) ? t2_ : t1_; }
    TR_STEP(8, XOR8, 8) TR_STEP(4, XOR7, 4) TR_STEP(2, XOR2, 2) TR_STEP(1, XOR1, 1)
    const float sc = x[0] * C2;
    float pm = sc; pm = fmaxf(pm, dpp<ROR4>(pm)); pm = fmaxf(pm, dpp<XOR8>(pm)); pm = xrow16_max(pm);
    const float mn = fmaxf(m, pm);
    const float alpha = __builtin_amdgcn_exp2f(m - mn), p = __builtin_amdgcn_exp2f(sc - mn);
    l = fmaf(l, alpha, p); m = mn;
#pragma unroll
    for (int q4 = 0; q4 < 4; ++q4) {
      const float al = q4 == 0 ? dpp<0x00>(alpha) : q4 == 1 ? dpp<0x55>(alpha) : q4 == 2 ? dpp<0xAA>(alpha) : dpp<0xFF>(alpha);
#pragma unroll
      for (int k = 0; k < 8; ++k) acc[q4 * 8 + k] *= al; }
    float* prow = lds + (wave * 4 + r) * 16;
    prow[c] = p;
    asm volatile("s_waitcnt lgkmcnt(0)" ::: "memory");
    f32x4 pr[4];
#pragma unroll
    for (int u = 0; u < 4; ++u) pr[u] = *reinterpret_cast<const f32x4*>(prow + 4 * u);
#pragma unroll
    for (int t = 0; t < 4; ++t) {
      float vf[8];
#pragma unroll
      for (int k = 0; k < 4; ++k) { vf[2 * k] = __builtin_bit_cast(float, Vc[t][k] << 16); vf[2 * k + 1] = __builtin_bit_cast(float, Vc[t][k] & 0xffff0000u); }
#pragma unroll
      for (int g = 0; g < 4; ++g) { const float pg_ = pr[t][g];
#pragma unroll
        for (int k = 0; k < 8; ++k) acc[g * 8 + k] = fmaf(pg_, vf[k], acc[g * 8 + k]); } }
  }
#pragma unroll
  for (int k = 0; k < 32; ++k) acc[k] = xrow16_sum(acc[k]);
  { float t = l; t += dpp<ROR4>(t); t += dpp<XOR8>(t); l = xrow16_sum(t); }
  float* mrg = lds + WAVES * 64;
  if (r == 0) {
#pragma unroll
    for (int g = 0; g < 4; ++g) {
      float* rec = mrg + (wave * 4 + g) * MRG_REC;
#pragma unroll
      for (int k = 0; k < 8; k += 4) *reinterpret_cast<f32x4*>(rec + 4 + 8 * c + k) = f32x4{acc[g * 8 + k], acc[g * 8 + k + 1], acc[g * 8 + k + 2], acc[g * 8 + k + 3]};
      if (c == g) { rec[0] = m; rec[1] = l; } } }
  __syncthreads();
  {
    const int g = wave, d0 = 2 * lane;
    const size_t row = (size_t)b * HQ + kvh * 4 + g;
    float M = -INFINITY;
#pragma unroll
    for (int w = 0; w < WAVES; ++w) M = fmaxf(M, mrg[(w * 4 + g) * MRG_REC]);
    float L = 0.f, o0 = 0.f, o1 = 0.f;
#pragma unroll
    for (int w = 0; w < WAVES; ++w) { const float* rec = mrg + (w * 4 + g) * MRG_REC;
      const float wt = __builtin_amdgcn_exp2f(rec[0] - M);
      L = fmaf(wt, rec[1], L); o0 = fmaf(wt, rec[4 + d0], o0); o1 = fmaf(wt, rec[4 + d0 + 1], o1); }
    const size_t slot = row * NSPLIT + s;
    *(float2*)(po + slot * DH + d0) = make_float2(o0, o1);
    if (lane == 0) *(float2*)(pml + 2 * slot) = make_float2(M, L);
  }
}

__global__ void __launch_bounds__(256) k_oproj(const float* pml, const float* po, const unsigned short* wo, float* part) {
  __shared__ float o[8][128];
  const int h = blockIdx.x, n = blockIdx.y, tid = threadIdx.x;
  for (int e = tid; e < 1024; e += 256) {
    const int b = e >> 7, d = e & 127;
    const size_t row = (size_t)b * HQ + h;
    float M = -INFINITY;
    for (int s = 0; s < NSPLIT; ++s) M = fmaxf(M, pml[2 * (row * NSPLIT + s)]);
    float L = 0, a = 0;
    for (int s = 0; s < NSPLIT; ++s) { float wt = __builtin_amdgcn_exp2f(pml[2 * (row * NSPLIT + s)] - M); L = fmaf(wt, pml[2 * (row * NSPLIT + s) + 1], L); a = fmaf(wt, po[(row * NSPLIT + s) * DH + d], a); }
    o[b][d] = bf2f(f2bf(a / L));
  }
  __syncthreads();
  const int col = n * 64 + (tid & 63), bq = tid >> 6;
  float a0 = 0, a1 = 0;
  for (int d = 0; d < 128; ++d) { float wv = bf2f(wo[(size_t)(h * 128 + d) * 512 + col]); a0 = fmaf(o[2 * bq][d], wv, a0); a1 = fmaf(o[2 * bq + 1][d], wv, a1); }
  part[((size_t)h * 8 + 2 * bq) * 512 + col] = a0;
  part[((size_t)h * 8 + 2 * bq + 1) * 512 + col] = a1;
}

__global__ void __launch_bounds__(512) k_final(const float* part, const unsigned short* x, const unsigned short* w1, const unsigned short* b1, const unsigned short* w2, const unsigned short* b2, unsigned short* out) {
  __shared__ float hs[512];
  __shared__ float red[16];
  const int b = blockIdx.y, j = blockIdx.x, tid = threadIdx.x, lane = tid & 63, w = tid >> 6;
  float a = 0;
  for (int h = 0; h < 32; ++h) a += part[((size_t)h * 8 + b) * 512 + tid];
  float hv = bf2f(x[b * 512 + tid]) + a;
  hs[tid] = hv;
  float s = wave_sum(hv); if (lane == 0) red[w] = s;
  __syncthreads();
  float m = 0; for (int i = 0; i < 8; ++i) m += red[i]; m *= (1.f / 512);
  float d = hv - m; float ss = wave_sum(d * d); if (lane == 0) red[8 + w] = ss;
  __syncthreads();
  float var = 0; for (int i = 0; i < 8; ++i) var += red[8 + i]; var *= (1.f / 512);
  float r1 = 1.f / sqrtf(var + 1e-5f);
  float var2 = var * r1 * r1;
  float r2 = 1.f / sqrtf(var2 + 1e-5f);
  for (int e = 0; e < 2; ++e) {
    const int dd = j * 16 + w * 2 + e;
    const float hp = (hs[dd] - m) * r1 * r2;
    float acc = 0;
    for (int f = lane; f < FF; f += 64) {
      float u = fmaf(hp, bf2f(w1[f]), bf2f(b1[f]));
      float z = u * (0.7978845608028654f + 0.7978845608028654f * 0.044715f * u * u);
      float g = u / (1.f + __expf(-2.f * z));
      acc = fmaf(g, bf2f(w2[f]), acc);
    }
    acc = wave_sum(acc);
    if (lane == 0) out[b * 512 + dd] = f2bf(hs[dd] + acc + bf2f(b2[0]));
  }
}

extern "C" void kernel_launch(void* const* d_in, const int* in_sizes, int n_in, void* d_out, int out_size, void* d_ws, size_t ws_size, hipStream_t stream) {
  const unsigned short* x = (const unsigned short*)d_in[0];
  const unsigned short* kc = (const unsigned short*)d_in[1];
  const unsigned short* vc = (const unsigned short*)d_in[2];
  const unsigned short* wq = (const unsigned short*)d_in[3];
  const unsigned short* wo = (const unsigned short*)d_in[4];
  const unsigned short* w1 = (const unsigned short*)d_in[6];
  const unsigned short* b1 = (const unsigned short*)d_in[7];
  const unsigned short* w2 = (const unsigned short*)d_in[8];
  const unsigned short* b2 = (const unsigned short*)d_in[9];
  char* ws = (char*)d_ws;
  unsigned short* q = (unsigned short*)ws;
  float* pml = (float*)(ws + 65536);
  float* po = (float*)(ws + 131072);
  float* part = (float*)(ws + 131072 + 1048576);
  k_q<<<256, 512, 0, stream>>>(x, wq, q);
  k_attn<<<dim3(NSPLIT, HKV, B), 256, 0, stream>>>(q, kc, vc, pml, po);
  k_oproj<<<dim3(32, 8), 256, 0, stream>>>(pml, po, wo, part);
  k_final<<<dim3(32, 8), 512, 0, stream>>>(part, x, w1, b1, w2, b2, (unsigned short*)d_out);
}
```

```cpp
#include <hip/hip_runtime.h>
#include <cstdint>

#define GAS __attribute__((address_space(1)))
#define LAS __attribute__((address_space(3)))
#define RLX_AGENT __ATOMIC_RELAXED, __HIP_MEMORY_SCOPE_AGENT
typedef unsigned u32x4 __attribute__((ext_vector_type(4)));
typedef unsigned u32x2 __attribute__((ext_vector_type(2)));
typedef float f32x4 __attribute__((ext_vector_type(4)));
typedef float f32x2 __attribute__((ext_vector_type(2)));
typedef __bf16 bf16x8 __attribute__((ext_vector_type(8)));
typedef __bf16 bf16x2 __attribute__((ext_vector_type(2)));
typedef short s16x4 __attribute__((ext_vector_type(4)));
typedef unsigned long long u64;

#define STAMPS 0
#define PACE 6
#define TFIRST 400
#define W_NT 1
#define SL1 2
#define SL2 2
#define SL3 1

constexpr int NWG = 256, NTHR = 512, NW = 8, NSW = 7;
constexpr int SK = 8192, DH = 128, FF = 1792;
constexpr int NTW = 128;
constexpr unsigned WS_EPOCH = 0, WS_FLAGS = 4096, WS_QG = 8192;
constexpr unsigned WS_REC = 0x50000, SL_B = 528, REC_B = 4 * SL_B;
constexpr unsigned WS_OB = 0xD8000;
constexpr unsigned WS_ANS = 0xE8000;
constexpr unsigned WS_END = 0x108000;
static_assert(WS_REC + 256 * REC_B <= WS_OB, "ws");
constexpr unsigned WS_STAMP = 1u << 24;
constexpr unsigned SENT = 0xFFFFFFFFu;
constexpr int AUX_SC1 = 16;
constexpr unsigned SPIN_CAP = 1u << 16;
constexpr int L_WQ = 0;
constexpr int L_WO = 16384;
constexpr int L_MIX = 32768;
constexpr int L_MRG = L_MIX + 11264;
constexpr int L_X = L_MRG;
constexpr int L_Q = L_MRG + 16640;
constexpr int L_H = L_Q + 1024;
constexpr int L_ST = L_H + 2048;
constexpr int L_END = L_ST + 256;

__device__ __forceinline__ float bf2f(unsigned b) { return __builtin_bit_cast(float, b << 16); }
__device__ __forceinline__ float bflo(unsigned v) { return __builtin_bit_cast(float, v << 16); }
__device__ __forceinline__ float bfhi(unsigned v) { return __builtin_bit_cast(float, v & 0xffff0000u); }
__device__ __forceinline__ unsigned f2bf(float f) { return (unsigned)__builtin_bit_cast(unsigned short, (__bf16)f); }
__device__ __forceinline__ unsigned pk2bf(float lo, float hi) { bf16x2 v = {(__bf16)lo, (__bf16)hi}; return __builtin_bit_cast(unsigned, v); }
template <int CTRL> __device__ __forceinline__ float dpp(float x) {
  return __builtin_bit_cast(float, __builtin_amdgcn_mov_dpp(__builtin_bit_cast(int, x), CTRL, 0xf, 0xf, true));
}
constexpr int XOR1 = 0xB1, XOR2 = 0x4E, XOR7 = 0x141, ROR4 = 0x124, ROR8 = 0x128, ROR12 = 0x12C;
__device__ __forceinline__ float xrow16_max(float x) {
  auto s = __builtin_amdgcn_permlane16_swap(__float_as_uint(x), __float_as_uint(x), false, false);
  x = fmaxf(__uint_as_float(s[0]), __uint_as_float(s[1]));
  auto t = __builtin_amdgcn_permlane32_swap(__float_as_uint(x), __float_as_uint(x), false, false);
  return fmaxf(__uint_as_float(t[0]), __uint_as_float(t[1]));
}
__device__ __forceinline__ float xrow16_sum(float x) {
  auto s = __builtin_amdgcn_permlane16_swap(__float_as_uint(x), __float_as_uint(x), false, false);
  x = __uint_as_float(s[0]) + __uint_as_float(s[1]);
  auto t = __builtin_amdgcn_permlane32_swap(__float_as_uint(x), __float_as_uint(x), false, false);
  return __uint_as_float(t[0]) + __uint_as_float(t[1]);
}
__device__ __forceinline__ float wave_sum(float x) {
  x += dpp<XOR1>(x); x += dpp<XOR2>(x); x += dpp<XOR7>(x); x += dpp<ROR8>(x);
  return xrow16_sum(x);
}
__device__ __forceinline__ float swap32(float x, int lane) {
  auto t = __builtin_amdgcn_permlane32_swap(__float_as_uint(x), __float_as_uint(x), false, false);
  return __uint_as_float(lane < 32 ? t[1] : t[0]);
}
__device__ __forceinline__ f32x4 mfma(u32x4 a, u32x4 b, f32x4 c) {
  return __builtin_amdgcn_mfma_f32_16x16x32_bf16(__builtin_bit_cast(bf16x8, a), __builtin_bit_cast(bf16x8, b), c, 0, 0, 0);
}
__device__ __forceinline__ f32x4 mfma16(u32x2 a, u32x2 b, f32x4 c) {
  return __builtin_amdgcn_mfma_f32_16x16x16bf16_1k(__builtin_bit_cast(s16x4, a), __builtin_bit_cast(s16x4, b), c, 0, 0, 0);
}
__device__ __forceinline__ void bar() { asm volatile("s_waitcnt lgkmcnt(0)\n\ts_barrier" ::: "memory"); }
__device__ __forceinline__ unsigned sanu(unsigned u) { return u == SENT ? 0x7FC07FC0u : u; }
__device__ __forceinline__ unsigned san(float f) { return sanu(__builtin_bit_cast(unsigned, f)); }
__device__ __forceinline__ void glds16(const GAS void* gsrc, unsigned lds_dst) {
  unsigned keep;
  asm volatile("s_mov_b32 %0, m0\n\ts_mov_b32 m0, %2\n\ts_nop 0\n\tglobal_load_lds_dwordx4 %1, off nt\n\ts_mov_b32 m0, %0" : "=&s"(keep) : "v"(gsrc), "s"(lds_dst) : "memory");
}
__device__ __forceinline__ u32x2 trread(LAS unsigned char* p) {
  return __builtin_bit_cast(u32x2, __builtin_amdgcn_ds_read_tr16_b64_v4i16((LAS s16x4*)p));
}
__device__ __forceinline__ unsigned ldw(const GAS unsigned char* p) { return __hip_atomic_load((const GAS unsigned*)p, RLX_AGENT); }
__device__ __forceinline__ void stw(GAS unsigned char* p, unsigned v) { __hip_atomic_store((GAS unsigned*)p, v, RLX_AGENT); }
__device__ __forceinline__ unsigned ldl(LAS unsigned char* p) { return __builtin_amdgcn_readfirstlane(*(volatile LAS unsigned*)p); }
#define PIN(x) asm volatile("" : "+v"(x))
#define STAMP(i) do {} while (0)
#define STAMPV(i, v) do {} while (0)

__global__ void __launch_bounds__(NTHR, 2) k_fused(const unsigned short* __restrict__ x_, const unsigned short* __restrict__ kc_, const unsigned short* __restrict__ vc_,
    const unsigned short* __restrict__ wq_, const unsigned short* __restrict__ wo_, const unsigned short* __restrict__ w1_, const unsigned short* __restrict__ b1_,
    const unsigned short* __restrict__ w2_, const unsigned short* __restrict__ b2_, unsigned short* out_, unsigned char* ws_) {
  __shared__ __attribute__((aligned(16))) unsigned char smem[L_END];
  LAS unsigned char* L = (LAS unsigned char*)smem;
  GAS unsigned char* ws = (GAS unsigned char*)ws_;
  const int tid = threadIdx.x, lane = tid & 63, w = __builtin_amdgcn_readfirstlane(tid >> 6);
  const int c = lane & 15, g = lane >> 4;
  const int xl = blockIdx.x & 7, y = blockIdx.x >> 3;
  const int s = xl >> 1, kvh = 2 * (y & 3) + ((xl ^ (xl >> 1)) & 1), b = y >> 2;
  const int pair = b * 8 + kvh;
  const int kvh2 = xl, cc = y;
  const __amdgpu_buffer_rsrc_t rs = __builtin_amdgcn_make_buffer_rsrc((void*)ws_, 0, (int)WS_END, 0x00020000);
  const size_t kvbase = ((size_t)pair * SK + (size_t)s * (NTW * 16)) * DH;
  const __amdgpu_buffer_rsrc_t rk = __builtin_amdgcn_make_buffer_rsrc((void*)(kc_ + kvbase), 0, (w == 7 ? NTW : NTW - 2) * 4096, 0x00020000);
  const __amdgpu_buffer_rsrc_t rv = __builtin_amdgcn_make_buffer_rsrc((void*)(vc_ + kvbase), 0, (w == 7 ? NTW : NTW - 2) * 4096, 0x00020000);
  const int koff = (c >> 2) * 256 + g * 64 + (c & 3) * 16 + w * 4096, voff = lane * 16 + w * 4096;
  constexpr float C2 = 0.088388347648318440f * 1.4426950408889634f;
  float m = -INFINITY;
  f32x4 acc[9];
#pragma unroll
  for (int e = 0; e < 9; ++e) acc[e] = f32x4{0.f, 0.f, 0.f, 0.f};
  const u32x2 ones = {0x3F803F80u, 0x3F803F80u};
#define ISSUE(R, RS, off, n) do { const int o_ = (off) + (n) * (NSW * 4096); \
    _Pragma("unroll") for (int i_ = 0; i_ < 4; ++i_) R[i_] = __builtin_amdgcn_raw_buffer_load_b128(RS, o_ + i_ * 1024, 0, 2); __builtin_amdgcn_sched_barrier(0); } while (0)
#define PROCESS(K, V, nn) do { \
    float sc_[4]; \
    _Pragma("unroll") for (int i_ = 0; i_ < 4; ++i_) { const f32x4 d_ = mfma(K[i_], qf, f32x4{0.f, 0.f, 0.f, 0.f}); \
      sc_[i_] = (d_[0] + dpp<ROR12>(d_[1])) + (dpp<ROR8>(d_[2]) + dpp<ROR4>(d_[3])); } \
    _Pragma("unroll") for (int i_ = 0; i_ < 4; ++i_) PIN(sc_[i_]); \
    __builtin_amdgcn_sched_barrier(0); \
    ISSUE(K, rk, koff, nn); \
    const float tm_ = xrow16_max(fmaxf(fmaxf(sc_[0], sc_[1]), fmaxf(sc_[2], sc_[3]))) * C2; \
    const float mn_ = fmaxf(m, tm_); \
    const float al_ = __builtin_amdgcn_exp2f(m - mn_); m = mn_; \
    u32x2 pa_; \
    _Pragma("unroll") for (int j_ = 0; j_ < 2; ++j_) pa_[j_] = pk2bf(__builtin_amdgcn_exp2f(fmaf(sc_[2 * j_], C2, -mn_)), __builtin_amdgcn_exp2f(fmaf(sc_[2 * j_ + 1], C2, -mn_))); \
    const float a0_ = __builtin_bit_cast(float, __builtin_amdgcn_readlane(__builtin_bit_cast(int, al_), 0)), a1_ = __builtin_bit_cast(float, __builtin_amdgcn_readlane(__builtin_bit_cast(int, al_), 1)), \
                a2_ = __builtin_bit_cast(float, __builtin_amdgcn_readlane(__builtin_bit_cast(int, al_), 2)), a3_ = __builtin_bit_cast(float, __builtin_amdgcn_readlane(__builtin_bit_cast(int, al_), 3)); \
    _Pragma("unroll") for (int e_ = 0; e_ < 9; ++e_) { acc[e_][0] *= a0_; acc[e_][1] *= a1_; acc[e_][2] *= a2_; acc[e_][3] *= a3_; } \
    acc[8] = mfma16(pa_, ones, acc[8]); \
    u32x2 bf_[8]; \
    _Pragma("unroll") for (int jj_ = 0; jj_ < 2; ++jj_) _Pragma("unroll") for (int d_ = 0; d_ < 4; ++d_) { \
      bf_[2 * d_][jj_] = __builtin_amdgcn_perm(V[2 * jj_ + 1][d_], V[2 * jj_][d_], 0x05040100u); \
      bf_[2 * d_ + 1][jj_] = __builtin_amdgcn_perm(V[2 * jj_ + 1][d_], V[2 * jj_][d_], 0x07060302u); } \
    _Pragma("unroll") for (int e_ = 0; e_ < 8; ++e_) PIN(bf_[e_]); \
    __builtin_amdgcn_sched_barrier(0); \
    ISSUE(V, rv, voff, nn); \
    _Pragma("unroll") for (int e_ = 0; e_ < 8; ++e_) acc[e_] = mfma16(pa_, bf_[e_], acc[e_]); \
    __builtin_amdgcn_sched_barrier(0); \
  } while (0)
  u32x4 wf[16];
  if (w == 7) {
    __builtin_amdgcn_s_setprio(3);
    const unsigned tstart = (unsigned)__builtin_amdgcn_s_memrealtime();
    u64 ep; unsigned b2v;
    asm volatile("global_load_dwordx2 %0, %1, off sc1" : "=v"(ep) : "v"(ws + WS_EPOCH) : "memory");
#pragma unroll
    for (int r = 0; r < 8; ++r) glds16((const GAS unsigned short*)x_ + r * 512 + lane * 8, (unsigned)(L_X + r * 1040));
    {
      const int c0 = (kvh * 4 + s) * 128 + b * 16;
      const GAS unsigned short* wp = (const GAS unsigned short*)wq_ + (size_t)(lane >> 1) * 4096 + c0 + (lane & 1) * 8;
#pragma unroll
      for (int i = 0; i < 16; ++i) glds16(wp + (size_t)i * 32 * 4096, (unsigned)(L_WQ + i * 1024));
    }
    {
      const u32x4 sv = {SENT, SENT, SENT, SENT};
      const unsigned rb = WS_REC + (unsigned)(pair * 4 + s) * REC_B;
      __builtin_amdgcn_raw_buffer_store_b128(sv, rs, rb + lane * 16, 0, AUX_SC1);
      __builtin_amdgcn_raw_buffer_store_b128(sv, rs, rb + 1024 + lane * 16, 0, AUX_SC1);
      const int l2 = (lane - 20) & 31;
      const unsigned o3 = lane < 4 ? rb + 2048 + lane * 16
                        : lane < 20 ? WS_OB + (unsigned)(b * 32 + kvh * 4 + s) * 256 + (lane - 4) * 16
                        : WS_ANS + (unsigned)(((l2 >> 2) * 8 + kvh2) * 512 + cc * 16) * 4 + (l2 & 3) * 16;
      __builtin_amdgcn_raw_buffer_store_b128(sv, rs, o3, 0, AUX_SC1);
    }
    if (lane == 0) { *(volatile LAS unsigned*)(L + L_ST + 128) = 0u; *(volatile LAS unsigned*)(L + L_ST + 136) = 0u; }
    bar();
    STAMP(0);
    asm volatile("s_waitcnt vmcnt(19)" : "+v"(ep) :: "memory");
    const u64 tag = ep * 6364136223846793005ull + 1442695040888963407ull;
    const unsigned tagq = (unsigned)(tag >> 32);
    const int ta = (8 * g + (c >> 2)) * 32 + (c & 3) * 8;
    {
      u32x4 xa[16];
#pragma unroll
      for (int ks = 0; ks < 16; ++ks) xa[ks] = *(LAS u32x4*)(L + L_X + (c & 7) * 1040 + (32 * ks + 8 * g) * 2);
      {
        const u32x4 xr = *(LAS u32x4*)(L + L_X + b * 1040 + lane * 16);
        *(LAS f32x4*)(L + L_H + lane * 32) = f32x4{bflo(xr[0]), bfhi(xr[0]), bflo(xr[1]), bfhi(xr[1])};
        *(LAS f32x4*)(L + L_H + lane * 32 + 16) = f32x4{bflo(xr[2]), bfhi(xr[2]), bflo(xr[3]), bfhi(xr[3])};
      }
      f32x4 ga0 = {0.f, 0.f, 0.f, 0.f}, ga1 = ga0;
#pragma unroll
      for (int ks = 0; ks < 16; ++ks) {
        const u32x4 xb = xa[ks];
        if (c == 8) xa[ks] = u32x4{0x3F803F80u, 0x3F803F80u, 0x3F803F80u, 0x3F803F80u};
        if (ks & 1) ga1 = mfma(xa[ks], xb, ga1); else ga0 = mfma(xa[ks], xb, ga0);
      }
      const f32x4 ga = ga0 + ga1;
      const float dg = (c & 2) ? ((c & 1) ? ga[3] : ga[2]) : ((c & 1) ? ga[1] : ga[0]);
      LAS float* stt = (LAS float*)(L + L_ST + 64);
      if (c < 8 && g == (c >> 2)) stt[c] = dg;
      if (c < 8 && g == 2) stt[8 + c] = ga[0];
      asm volatile("s_waitcnt vmcnt(3) lgkmcnt(0)" ::: "memory");
      STAMP(1);
      f32x4 qa[4];
#pragma unroll
      for (int i = 0; i < 4; ++i) qa[i] = f32x4{0.f, 0.f, 0.f, 0.f};
#pragma unroll
      for (int ks = 0; ks < 16; ++ks) {
        const u32x2 b0 = trread(L + L_WQ + ta + ks * 1024), b1 = trread(L + L_WQ + ta + ks * 1024 + 128);
        qa[ks & 3] = mfma(xa[ks], u32x4{b0[0], b0[1], b1[0], b1[1]}, qa[ks & 3]);
      }
      const f32x4 ssq = *(LAS f32x4*)(L + L_ST + 64 + (g & 1) * 16), ssm = *(LAS f32x4*)(L + L_ST + 96 + (g & 1) * 16);
      const f32x4 qq = (qa[0] + qa[1]) + (qa[2] + qa[3]);
      const float cs = __shfl(qq[0], 32 + c);
      float qv[4];
#pragma unroll
      for (int i = 0; i < 4; ++i) {
        const float mean = ssm[i] * (1.f / 512);
        const float var = fmaxf(ssq[i] * (1.f / 512) - mean * mean, 0.f);
        qv[i] = (qq[i] - mean * cs) * __builtin_amdgcn_rsqf(var + 1e-5f);
      }
      unsigned pk[4];
#pragma unroll
      for (int i = 0; i < 4; ++i) { const float nb = dpp<XOR1>(qv[i]); pk[i] = (c & 1) ? pk2bf(nb, qv[i]) : pk2bf(qv[i], nb); }
      const unsigned pa = (c & 1) ? pk[2] : pk[0], pb = (c & 1) ? pk[3] : pk[1];
      const unsigned pbs = __builtin_bit_cast(unsigned, swap32(__builtin_bit_cast(float, pb), lane));
      const int bq = 4 * (g & 1) + 2 * (c & 1) + (g >> 1);
      __hip_atomic_store((GAS u64*)(ws + WS_QG) + (kvh * 8 + bq) * 256 + s * 64 + b * 8 + (c >> 1), ((u64)tagq << 32) | (g < 2 ? pa : pbs), RLX_AGENT);
    }
    STAMP(2);
    for (int sp = 0; sp < 1000 && (unsigned)__builtin_amdgcn_s_memrealtime() - tstart < (unsigned)TFIRST; ++sp) __builtin_amdgcn_s_sleep(2);
    {
      u32x4 v0, v1;
      unsigned spins = 0;
      for (;; ++spins) {
        v0 = __builtin_amdgcn_raw_buffer_load_b128(rs, WS_QG + (unsigned)(kvh * 8 + b) * 2048 + lane * 16, 0, AUX_SC1);
        v1 = __builtin_amdgcn_raw_buffer_load_b128(rs, WS_QG + (unsigned)(kvh * 8 + b) * 2048 + 1024 + lane * 16, 0, AUX_SC1);
        const bool ok = (v0[1] == tagq) & (v0[3] == tagq) & (v1[1] == tagq) & (v1[3] == tagq);
        if (__all(ok) || spins > SPIN_CAP) break;
        asm volatile("" ::: "memory");
      }
      *(LAS u32x2*)(L + L_Q + lane * 8) = u32x2{v0[0], v0[2]};
      *(LAS u32x2*)(L + L_Q + 512 + lane * 8) = u32x2{v1[0], v1[2]};
      asm volatile("s_waitcnt lgkmcnt(0)" ::: "memory");
      if (lane == 0) *(volatile LAS unsigned*)(L + L_ST + 136) = 1u;
      STAMPV(12, spins);
    }
    STAMP(3);
    if (lane == 0) __hip_atomic_store((GAS u64*)(ws + WS_FLAGS) + blockIdx.x, tag, RLX_AGENT);
    {
      const GAS unsigned short* wp = (const GAS unsigned short*)wo_ + (size_t)(kvh2 * 512 + (lane >> 1)) * 512 + cc * 16 + (lane & 1) * 8;
#pragma unroll
      for (int i = 0; i < 16; ++i) glds16(wp + (size_t)i * 32 * 512, (unsigned)(L_WO + i * 1024));
#pragma unroll
      for (int i = 0; i < 11; ++i) {
        const int e = min(i * 64 + lane, 671);
        const GAS unsigned short* src = e < 224 ? (const GAS unsigned short*)w1_ + e * 8 : e < 448 ? (const GAS unsigned short*)b1_ + (e - 224) * 8 : (const GAS unsigned short*)w2_ + (e - 448) * 8;
        glds16(src, (unsigned)(L_MIX + i * 1024));
      }
    }
    asm volatile("global_load_ushort %0, %1, off" : "=v"(b2v) : "v"((const GAS unsigned short*)b2_) : "memory");
    asm volatile("s_waitcnt vmcnt(0)" : "+v"(b2v) :: "memory");
    if (lane == 0) *(LAS float*)(L + L_ST + 132) = bf2f(b2v);
#pragma unroll
    for (int ks = 0; ks < 16; ++ks) {
      const u32x2 b0 = trread(L + L_WO + ta + ks * 1024), b1 = trread(L + L_WO + ta + ks * 1024 + 128);
      wf[ks] = u32x4{b0[0], b0[1], b1[0], b1[1]};
    }
    {
      u32x4 K0[4], V0[4];
      const u32x4 qf = *(LAS u32x4*)(L + L_Q + (c & 3) * 256 + (4 * g + (c >> 2)) * 16);
      ISSUE(K0, rk, koff, 17); ISSUE(V0, rv, voff, 17);
      for (int t = 0; t < 2; ++t) { const int koff2 = koff + 4096 * (1 + t), voff2 = voff + 4096 * (1 + t); { const int koff = koff2, voff = voff2; PROCESS(K0, V0, 17); } }
    }
    for (unsigned spins = 0; ldl(L + L_ST + 128) < (unsigned)NSW && spins < (1u << 22); ++spins) __builtin_amdgcn_s_sleep(4);
    {
      const GAS u64* fl = (const GAS u64*)(ws + WS_FLAGS) + lane;
      for (unsigned spins = 0;; ++spins) {
        bool ok = true;
        u64 v[4];
#pragma unroll
        for (int i = 0; i < 4; ++i) v[i] = __hip_atomic_load(fl + 64 * i, RLX_AGENT);
#pragma unroll
        for (int i = 0; i < 4; ++i) ok &= v[i] == tag;
        if (__all(ok) || spins > SPIN_CAP) break;
        __builtin_amdgcn_s_sleep(8);
      }
      if (blockIdx.x == 0 && lane == 0) __hip_atomic_store((GAS u64*)(ws + WS_EPOCH), tag, RLX_AGENT);
    }
    STAMP(11);
  } else {
    asm volatile("s_barrier" ::: "memory");
    u32x4 K0[4], V0[4], K1[4], V1[4], K2[4], V2[4], K3[4], V3[4];
#define PACEW() do { if (ldl(L + L_ST + 136) == 0u) asm volatile("s_waitcnt vmcnt(%0)" :: "i"(PACE) : "memory"); } while (0)
    ISSUE(K0, rk, koff, 0); ISSUE(V0, rv, voff, 0); PACEW(); ISSUE(K1, rk, koff, 1); PACEW(); ISSUE(V1, rv, voff, 1); PACEW();
    ISSUE(K2, rk, koff, 2); PACEW(); ISSUE(V2, rv, voff, 2); PACEW(); ISSUE(K3, rk, koff, 3); PACEW(); ISSUE(V3, rv, voff, 3);
    for (unsigned spins = 0; ldl(L + L_ST + 136) == 0u && spins < (1u << 24); ++spins) __builtin_amdgcn_s_sleep(1);
    asm volatile("" ::: "memory");
    const u32x4 qf = *(LAS u32x4*)(L + L_Q + (c & 3) * 256 + (4 * g + (c >> 2)) * 16);
    PROCESS(K0, V0, 4); PROCESS(K1, V1, 5);
    for (int n = 2; n < 18; n += 4) {
      PROCESS(K2, V2, n + 4); PROCESS(K3, V3, n + 5); PROCESS(K0, V0, n + 6); PROCESS(K1, V1, n + 7);
      if (n == 10 && lane == 0) __hip_atomic_fetch_add((LAS unsigned*)(L + L_ST + 128), 1u, __ATOMIC_RELAXED, __HIP_MEMORY_SCOPE_WORKGROUP);
    }
    __builtin_amdgcn_s_waitcnt(0x0F70);
    STAMP(4);
#pragma unroll
    for (int ks = 0; ks < 16; ++ks) wf[ks] = u32x4{0u, 0u, 0u, 0u};
  }
  {
    LAS float* rec = (LAS float*)(L + L_MRG) + w * 520;
    if (g == 0) {
#pragma unroll
      for (int r = 0; r < 4; ++r) {
        *(LAS f32x4*)(rec + r * 128 + 8 * c) = f32x4{acc[0][r], acc[1][r], acc[2][r], acc[3][r]};
        *(LAS f32x4*)(rec + r * 128 + 8 * c + 4) = f32x4{acc[4][r], acc[5][r], acc[6][r], acc[7][r]};
      }
      if (c < 4) rec[512 + c] = m;
      if (c == 0) *(LAS f32x4*)(rec + 516) = acc[8];
    }
  }
  bar();
  const int hd = w >> 1, d = tid & 127;
  float oM, oL, oA;
  {
    const LAS float* rec = (const LAS float*)(L + L_MRG);
    float mw[NW];
#pragma unroll
    for (int ww = 0; ww < NW; ++ww) mw[ww] = rec[ww * 520 + 512 + hd];
    oM = mw[0];
#pragma unroll
    for (int ww = 1; ww < NW; ++ww) oM = fmaxf(oM, mw[ww]);
    oL = 0.f; oA = 0.f;
#pragma unroll
    for (int ww = 0; ww < NW; ++ww) { const float wt = __builtin_amdgcn_exp2f(mw[ww] - oM); oL = fmaf(wt, rec[ww * 520 + 516 + hd], oL); oA = fmaf(wt, rec[ww * 520 + tid], oA); }
    if (hd != s) {
      GAS unsigned char* rb = ws + WS_REC + (unsigned)(pair * 4 + s) * REC_B + hd * SL_B;
      stw(rb + d * 4, san(oA));
      if (d < 2) stw(rb + 512 + d * 4, san(d ? oL : oM));
    }
  }
  STAMP(5);
  if (hd == s) {
    unsigned pa[3], pm[3], pl[3];
#pragma unroll
    for (int j = 0; j < 3; ++j) { pa[j] = SENT; pm[j] = SENT; pl[j] = SENT; }
    for (unsigned spins = 0;; ++spins) {
      bool ok = true;
#pragma unroll
      for (int j = 0; j < 3; ++j) {
        const GAS unsigned char* rb = ws + WS_REC + (unsigned)(pair * 4 + ((s + 1 + j) & 3)) * REC_B + s * SL_B;
        if (!__all((pa[j] != SENT) & (pm[j] != SENT) & (pl[j] != SENT))) { pa[j] = ldw(rb + d * 4); pm[j] = ldw(rb + 512); pl[j] = ldw(rb + 516); }
      }
#pragma unroll
      for (int j = 0; j < 3; ++j) ok &= (pa[j] != SENT) & (pm[j] != SENT) & (pl[j] != SENT);
      if (__all(ok) || spins > SPIN_CAP) break;
      __builtin_amdgcn_s_sleep(SL1);
    }
    const float m0 = __builtin_bit_cast(float, pm[0]), m1 = __builtin_bit_cast(float, pm[1]), m2 = __builtin_bit_cast(float, pm[2]);
    const float M = fmaxf(fmaxf(oM, m0), fmaxf(m1, m2));
    const float w0 = __builtin_amdgcn_exp2f(oM - M), w1 = __builtin_amdgcn_exp2f(m0 - M), w2 = __builtin_amdgcn_exp2f(m1 - M), w3 = __builtin_amdgcn_exp2f(m2 - M);
    const float Ls = (w0 * oL + w1 * __builtin_bit_cast(float, pl[0])) + (w2 * __builtin_bit_cast(float, pl[1]) + w3 * __builtin_bit_cast(float, pl[2]));
    const float As = (w0 * oA + w1 * __builtin_bit_cast(float, pa[0])) + (w2 * __builtin_bit_cast(float, pa[1]) + w3 * __builtin_bit_cast(float, pa[2]));
    const float o = As * __builtin_amdgcn_rcpf(Ls);
    const float on = dpp<XOR1>(o);
    if (!(lane & 1)) stw(ws + WS_OB + (unsigned)(b * 32 + kvh * 4 + s) * 256 + (d >> 1) * 4, sanu(pk2bf(o, on)));
    STAMP(6);
  }
  if (w == 7) {
    u32x4 oa[8];
    const unsigned ob = WS_OB + (unsigned)((c & 7) * 32 + 4 * kvh2) * 256 + (unsigned)(256 * (c >> 3) + 8 * g) * 2;
    bool okl = false;
    for (unsigned spins = 0;; ++spins) {
      if (!okl) {
#pragma unroll
        for (int t = 0; t < 8; ++t) oa[t] = __builtin_amdgcn_raw_buffer_load_b128(rs, ob + t * 64, 0, AUX_SC1);
      }
      okl = true;
#pragma unroll
      for (int t = 0; t < 8; ++t) okl &= (oa[t][0] != SENT) & (oa[t][1] != SENT) & (oa[t][2] != SENT) & (oa[t][3] != SENT);
      if (__all(okl) || spins > SPIN_CAP) break;
      __builtin_amdgcn_s_sleep(SL2);
      asm volatile("" ::: "memory");
    }
    f32x4 a0 = {0.f, 0.f, 0.f, 0.f}, a1 = a0, a2 = a0, a3 = a0;
#pragma unroll
    for (int t = 0; t < 8; t += 2) {
      a0 = mfma(oa[t], wf[t], a0); a1 = mfma(oa[t], wf[t + 8], a1);
      a2 = mfma(oa[t + 1], wf[t + 1], a2); a3 = mfma(oa[t + 1], wf[t + 9], a3);
    }
    const f32x4 lo = a0 + a2, hi = a1 + a3;
    const float r0 = swap32(g < 2 ? lo[2] : hi[0], lane), r1 = swap32(g < 2 ? lo[3] : hi[1], lane);
    const float e0 = (g < 2 ? lo[0] : hi[2]) + r0, e1 = (g < 2 ? lo[1] : hi[3]) + r1;
    GAS unsigned char* ap = ws + WS_ANS + (unsigned)(((4 * (g & 1) + 2 * (g >> 1)) * 8 + kvh2) * 512 + cc * 16 + c) * 4;
    stw(ap, san(e0)); stw(ap + 8 * 512 * 4, san(e1));
    STAMP(7);
  }
  f32x2 pw1[14], pb1[14], pw2[14];
#pragma unroll
  for (int i = 0; i < 7; ++i) {
    const u32x2 a = *(LAS u32x2*)(L + L_MIX + (i * 256 + lane * 4) * 2);
    const u32x2 bb = *(LAS u32x2*)(L + L_MIX + 3584 + (i * 256 + lane * 4) * 2);
    const u32x2 cc2 = *(LAS u32x2*)(L + L_MIX + 7168 + (i * 256 + lane * 4) * 2);
    pw1[2 * i] = f32x2{bflo(a[0]), bfhi(a[0])}; pw1[2 * i + 1] = f32x2{bflo(a[1]), bfhi(a[1])};
    pb1[2 * i] = f32x2{bflo(bb[0]), bfhi(bb[0])}; pb1[2 * i + 1] = f32x2{bflo(bb[1]), bfhi(bb[1])};
    pw2[2 * i] = f32x2{bflo(cc2[0]), bfhi(cc2[0])}; pw2[2 * i + 1] = f32x2{bflo(cc2[1]), bfhi(cc2[1])};
  }
#pragma unroll
  for (int i = 0; i < 14; ++i) { PIN(pw1[i]); PIN(pb1[i]); PIN(pw2[i]); }
  asm volatile("s_barrier" ::: "memory");
  {
    unsigned av[8];
    for (unsigned spins = 0;; ++spins) {
#pragma unroll
      for (int k = 0; k < 8; ++k) av[k] = ldw(ws + WS_ANS + (unsigned)((b * 8 + k) * 512 + tid) * 4);
      bool ok = true;
#pragma unroll
      for (int k = 0; k < 8; ++k) ok &= av[k] != SENT;
      if (__all(ok) || spins > SPIN_CAP) break;
      __builtin_amdgcn_s_sleep(SL3);
    }
    const float a = ((__builtin_bit_cast(float, av[0]) + __builtin_bit_cast(float, av[1])) + (__builtin_bit_cast(float, av[2]) + __builtin_bit_cast(float, av[3])))
                  + ((__builtin_bit_cast(float, av[4]) + __builtin_bit_cast(float, av[5])) + (__builtin_bit_cast(float, av[6]) + __builtin_bit_cast(float, av[7])));
    const float hv = *(LAS float*)(L + L_H + tid * 4) + a;
    *(LAS float*)(L + L_H + tid * 4) = hv;
    const float mw = wave_sum(hv) * (1.f / 64);
    const float dd = hv - mw;
    const float m2 = wave_sum(dd * dd);
    if (lane == 0) *(LAS f32x2*)(L + L_ST + w * 8) = f32x2{mw, m2};
  }
  bar();
  STAMP(8);
  {
    float st[16];
#pragma unroll
    for (int ww = 0; ww < 4; ++ww) { const f32x4 v = *(LAS f32x4*)(L + L_ST + ww * 16); st[4 * ww] = v[0]; st[4 * ww + 1] = v[1]; st[4 * ww + 2] = v[2]; st[4 * ww + 3] = v[3]; }
    const int e0 = (kvh * 4 + s) * 16 + 2 * w;
    const f32x2 hh = *(LAS f32x2*)(L + L_H + e0 * 4);
    const float bb = *(LAS float*)(L + L_ST + 132);
    const float mean = (((st[0] + st[2]) + (st[4] + st[6])) + ((st[8] + st[10]) + (st[12] + st[14]))) * 0.125f;
    float dq[8];
#pragma unroll
    for (int ww = 0; ww < 8; ++ww) { const float dd = st[2 * ww] - mean; dq[ww] = fmaf(64.f * dd, dd, st[2 * ww + 1]); }
    const float var = (((dq[0] + dq[1]) + (dq[2] + dq[3])) + ((dq[4] + dq[5]) + (dq[6] + dq[7]))) * (1.f / 512);
    const float r1 = __builtin_amdgcn_rsqf(var + 1e-5f);
    const float r2 = r1 * __builtin_amdgcn_rsqf(var * r1 * r1 + 1e-5f);
    const f32x2 hp = (hh - mean) * r2;
    constexpr float K0c = -2.f * 1.4426950408889634f * 0.7978845608028654f, K1c = K0c * 0.044715f;
    f32x2 ya = {0.f, 0.f}, yb = {0.f, 0.f};
#pragma unroll
    for (int i = 0; i < 14; ++i) {
#pragma unroll
      for (int e2 = 0; e2 < 2; ++e2) {
        const f32x2 u = pw1[i] * hp[e2] + pb1[i];
        const f32x2 t = (u * u) * K1c + K0c;
        const f32x2 z = u * t;
        f32x2 e = {__builtin_amdgcn_exp2f(z[0]), __builtin_amdgcn_exp2f(z[1])};
        e = e + 1.f;
        const f32x2 r = {__builtin_amdgcn_rcpf(e[0]), __builtin_amdgcn_rcpf(e[1])};
        if (e2) yb = (u * pw2[i]) * r + yb; else ya = (u * pw2[i]) * r + ya;
      }
    }
    const float y0 = wave_sum(ya[0] + ya[1]), y1 = wave_sum(yb[0] + yb[1]);
    if (lane == 0) *(GAS unsigned*)((GAS unsigned short*)out_ + b * 512 + e0) = pk2bf(hh[0] + (y0 + bb), hh[1] + (y1 + bb));
  }
  STAMP(9);
}

extern "C" void kernel_launch(void* const* d_in, const int* in_sizes, int n_in, void* d_out, int out_size, void* d_ws, size_t ws_size, hipStream_t stream) {
  k_fused<<<NWG, NTHR, 0, stream>>>((const unsigned short*)d_in[0], (const unsigned short*)d_in[1], (const unsigned short*)d_in[2], (const unsigned short*)d_in[3],
      (const unsigned short*)d_in[4], (const unsigned short*)d_in[6], (const unsigned short*)d_in[7], (const unsigned short*)d_in[8], (const unsigned short*)d_in[9],
      (unsigned short*)d_out, (unsigned char*)d_ws);
}
```

```cpp
#include <hip/hip_runtime.h>
#include <cstdint>

#define GAS __attribute__((address_space(1)))
#define LAS __attribute__((address_space(3)))
#define RLX_AGENT __ATOMIC_RELAXED, __HIP_MEMORY_SCOPE_AGENT
typedef unsigned u32x4 __attribute__((ext_vector_type(4)));
typedef unsigned u32x2 __attribute__((ext_vector_type(2)));
typedef float f32x4 __attribute__((ext_vector_type(4)));
typedef float f32x2 __attribute__((ext_vector_type(2)));
typedef __bf16 bf16x8 __attribute__((ext_vector_type(8)));
typedef __bf16 bf16x2 __attribute__((ext_vector_type(2)));
typedef short s16x4 __attribute__((ext_vector_type(4)));
typedef unsigned long long u64;

#define STAMPS 0
#define TFIRST 550
#define GW 5
#define SL2 2
#define SL3 4
#define NT_Q 123
#define NT_O 121
#define DRY 0

constexpr int NWG = 256, NTHR = 512, NW = 8, NSW = 7;
constexpr int SK = 8192, DH = 128, FF = 1792;
constexpr int NT_B = (512 - NT_Q - NT_O) / 2;
static_assert(NT_Q >= 119 && NT_O >= 119 && NT_B >= 126 && NT_Q + NT_O + 2 * NT_B == 512, "tile split");
constexpr unsigned WS_EPOCH = 0, WS_FLAGS = 4096, WS_QG = 8192;
constexpr unsigned WS_REC = 0x50000, REC_B = 2176;
constexpr unsigned WS_ANS = 0xE8000;
constexpr unsigned WS_END = 0x108000;
static_assert(WS_REC + 256 * REC_B <= WS_ANS, "ws");
constexpr unsigned WS_STAMP = 1u << 24;
constexpr unsigned SENT = 0xFFFFFFFFu;
constexpr int AUX_SC1 = 16;
constexpr unsigned SPIN_CAP = 1u << 16;
constexpr int L_WT = 0;
constexpr int L_KV = 65536;
constexpr int L_R7 = L_KV + 7 * 8192;
constexpr int L_MIX = L_R7 + 2560;
constexpr int L_OA = L_MIX + 11264;
constexpr int L_Q = L_OA + 8320;
constexpr int L_H = L_Q + 1024;
constexpr int L_ST = L_H + 2048;
constexpr int L_END = L_ST + 768;
static_assert(L_END <= 163840, "lds");

__device__ __forceinline__ float bf2f(unsigned b) { return __builtin_bit_cast(float, b << 16); }
__device__ __forceinline__ float bflo(unsigned v) { return __builtin_bit_cast(float, v << 16); }
__device__ __forceinline__ float bfhi(unsigned v) { return __builtin_bit_cast(float, v & 0xffff0000u); }
__device__ __forceinline__ unsigned pk2bf(float lo, float hi) { bf16x2 v = {(__bf16)lo, (__bf16)hi}; return __builtin_bit_cast(unsigned, v); }
__device__ __forceinline__ float rbf(float x) { return (float)(__bf16)x; }
template <int CTRL> __device__ __forceinline__ float dpp(float x) {
  return __builtin_bit_cast(float, __builtin_amdgcn_mov_dpp(__builtin_bit_cast(int, x), CTRL, 0xf, 0xf, true));
}
constexpr int XOR1 = 0xB1, XOR2 = 0x4E, XOR7 = 0x141, ROR4 = 0x124, ROR8 = 0x128, ROR12 = 0x12C;
__device__ __forceinline__ float xrow16_max(float x) {
  auto s = __builtin_amdgcn_permlane16_swap(__float_as_uint(x), __float_as_uint(x), false, false);
  x = fmaxf(__uint_as_float(s[0]), __uint_as_float(s[1]));
  auto t = __builtin_amdgcn_permlane32_swap(__float_as_uint(x), __float_as_uint(x), false, false);
  return fmaxf(__uint_as_float(t[0]), __uint_as_float(t[1]));
}
__device__ __forceinline__ float xrow16_sum(float x) {
  auto s = __builtin_amdgcn_permlane16_swap(__float_as_uint(x), __float_as_uint(x), false, false);
  x = __uint_as_float(s[0]) + __uint_as_float(s[1]);
  auto t = __builtin_amdgcn_permlane32_swap(__float_as_uint(x), __float_as_uint(x), false, false);
  return __uint_as_float(t[0]) + __uint_as_float(t[1]);
}
__device__ __forceinline__ float wave_sum(float x) {
  x += dpp<XOR1>(x); x += dpp<XOR2>(x); x += dpp<XOR7>(x); x += dpp<ROR8>(x);
  return xrow16_sum(x);
}
__device__ __forceinline__ float swap32(float x, int lane) {
  auto t = __builtin_amdgcn_permlane32_swap(__float_as_uint(x), __float_as_uint(x), false, false);
  return __uint_as_float(lane < 32 ? t[1] : t[0]);
}
__device__ __forceinline__ f32x4 mfma(u32x4 a, u32x4 b, f32x4 c) {
  return __builtin_amdgcn_mfma_f32_16x16x32_bf16(__builtin_bit_cast(bf16x8, a), __builtin_bit_cast(bf16x8, b), c, 0, 0, 0);
}
__device__ __forceinline__ f32x4 mfma16(u32x2 a, u32x2 b, f32x4 c) {
  return __builtin_amdgcn_mfma_f32_16x16x16bf16_1k(__builtin_bit_cast(s16x4, a), __builtin_bit_cast(s16x4, b), c, 0, 0, 0);
}
__device__ __forceinline__ void bar() { asm volatile("s_waitcnt lgkmcnt(0)\n\ts_barrier" ::: "memory"); }
__device__ __forceinline__ unsigned san(float f) { const unsigned u = __builtin_bit_cast(unsigned, f); return u == SENT ? 0x7FC00000u : u; }
__device__ __forceinline__ void glds16(const GAS void* gsrc, unsigned lds_dst) {
  unsigned keep;
  asm volatile("s_mov_b32 %0, m0\n\ts_mov_b32 m0, %2\n\ts_nop 0\n\tglobal_load_lds_dwordx4 %1, off nt\n\ts_mov_b32 m0, %0" : "=&s"(keep) : "v"(gsrc), "s"(lds_dst) : "memory");
}
__device__ __forceinline__ void glds16d(const GAS void* gsrc, unsigned lds_dst) {
  unsigned keep;
  asm volatile("s_mov_b32 %0, m0\n\ts_mov_b32 m0, %2\n\ts_nop 0\n\tglobal_load_lds_dwordx4 %1, off\n\ts_mov_b32 m0, %0" : "=&s"(keep) : "v"(gsrc), "s"(lds_dst) : "memory");
}
__device__ __forceinline__ u32x2 trread(LAS unsigned char* p) {
  return __builtin_bit_cast(u32x2, __builtin_amdgcn_ds_read_tr16_b64_v4i16((LAS s16x4*)p));
}
__device__ __forceinline__ unsigned ldw(const GAS unsigned char* p) { return __hip_atomic_load((const GAS unsigned*)p, RLX_AGENT); }
__device__ __forceinline__ void stw(GAS unsigned char* p, unsigned v) { __hip_atomic_store((GAS unsigned*)p, v, RLX_AGENT); }
__device__ __forceinline__ unsigned ldl(LAS unsigned char* p) { return __builtin_amdgcn_readfirstlane(*(volatile LAS unsigned*)p); }
__device__ __forceinline__ u64 next_tag(u64 ep) { return ep * 6364136223846793005ull + 1442695040888963407ull; }
__device__ __forceinline__ bool full4(u32x4 v) { return (v[0] != SENT) & (v[1] != SENT) & (v[2] != SENT) & (v[3] != SENT); }
#define PIN(x) asm volatile("" : "+v"(x))
#define STAMP(i) do {} while (0)
#define STAMPV(i, v) do {} while (0)

__global__ void __launch_bounds__(NTHR, 2) k_fused(const unsigned short* __restrict__ x_, const unsigned short* __restrict__ kc_, const unsigned short* __restrict__ vc_,
    const unsigned short* __restrict__ wq_, const unsigned short* __restrict__ wo_, const unsigned short* __restrict__ w1_, const unsigned short* __restrict__ b1_,
    const unsigned short* __restrict__ w2_, const unsigned short* __restrict__ b2_, unsigned short* out_, unsigned char* ws_) {
  __shared__ __attribute__((aligned(16))) unsigned char smem[L_END];
  LAS unsigned char* L = (LAS unsigned char*)smem;
  GAS unsigned char* ws = (GAS unsigned char*)ws_;
  const int tid = threadIdx.x, lane = tid & 63, w = __builtin_amdgcn_readfirstlane(tid >> 6);
  const int c = lane & 15, g = lane >> 4;
  const int xl = blockIdx.x & 7, y = blockIdx.x >> 3;
  const int s = (xl + y) & 3, kvh = 2 * (y & 3) + (xl >> 2), b = y >> 2;
  const int pair = b * 8 + kvh;
  const __amdgpu_buffer_rsrc_t rs = __builtin_amdgcn_make_buffer_rsrc((void*)ws_, 0, (int)WS_END, 0x00020000);
  const unsigned tstart = (unsigned)__builtin_amdgcn_s_memrealtime();
  STAMP(0);
  u64 ep = 0;
  if (w == 7 || (s == 0 && w < 4)) asm volatile("global_load_dwordx2 %0, %1, off sc1" : "=v"(ep) : "v"(ws + WS_EPOCH) : "memory");
  if (w == 7) {
    if (lane == 0) { *(volatile LAS unsigned*)(L + L_ST + 128) = 0u; *(volatile LAS unsigned*)(L + L_ST + 136) = 0u; }
    const u32x4 sv = {SENT, SENT, SENT, SENT};
    const unsigned rb = WS_REC + (unsigned)(pair * 4 + s) * REC_B;
    __builtin_amdgcn_raw_buffer_store_b128(sv, rs, rb + lane * 16, 0, AUX_SC1);
    __builtin_amdgcn_raw_buffer_store_b128(sv, rs, rb + 1024 + lane * 16, 0, AUX_SC1);
    if (lane < 2) __builtin_amdgcn_raw_buffer_store_b128(sv, rs, rb + 2048 + lane * 16, 0, AUX_SC1);
    if (s == 1) {
      const unsigned oa = WS_ANS + (unsigned)((g * 8 + (pair >> 3)) * 512 + (pair & 7) * 64) * 4 + c * 16;
      __builtin_amdgcn_raw_buffer_store_b128(sv, rs, oa, 0, AUX_SC1);
      __builtin_amdgcn_raw_buffer_store_b128(sv, rs, oa + 4 * 8 * 512 * 4, 0, AUX_SC1);
    }
  }
  if (s == 1) {
    const GAS unsigned short* wp = (const GAS unsigned short*)wo_ + (size_t)((pair >> 3) * 512 + 64 * w + (lane >> 3)) * 512 + (pair & 7) * 64 + (lane & 7) * 8;
#pragma unroll
    for (int i = 0; i < 8; ++i) glds16(wp + (size_t)i * 8 * 512, (unsigned)(L_WT + (w * 8 + i) * 1024));
  }
  if (s == 0) {
    u32x4 xa[2];
    {
      const GAS unsigned short* xp = (const GAS unsigned short*)x_ + (c & 7) * 512 + 64 * w + 8 * g;
      asm volatile("global_load_dwordx4 %0, %2, off\n\tglobal_load_dwordx4 %1, %2, off offset:64" : "=&v"(xa[0]), "=&v"(xa[1]) : "v"(xp) : "memory");
    }
    const GAS unsigned short* wp = (const GAS unsigned short*)wq_ + (size_t)(64 * w + (lane >> 3)) * 4096 + pair * 64 + (lane & 7) * 8;
#pragma unroll
    for (int i = 0; i < 8; ++i) glds16(wp + (size_t)i * 8 * 4096, (unsigned)(L_WT + (w * 8 + i) * 1024));
    asm volatile("s_waitcnt vmcnt(8)" : "+v"(xa[0]), "+v"(xa[1]), "+v"(ep) :: "memory");
    {
      f32x4 ga = {0.f, 0.f, 0.f, 0.f};
#pragma unroll
      for (int t = 0; t < 2; ++t) ga = mfma(c == 8 ? u32x4{0x3F803F80u, 0x3F803F80u, 0x3F803F80u, 0x3F803F80u} : xa[t], xa[t], ga);
      const float dg = (c & 2) ? ((c & 1) ? ga[3] : ga[2]) : ((c & 1) ? ga[1] : ga[0]);
      LAS float* sp = (LAS float*)(L + L_ST + 256) + w * 16;
      if (c < 8 && g == (c >> 2)) sp[2 * c] = dg;
      if (c < 8 && g == 2) sp[2 * c + 1] = ga[0];
    }
    bar();
    {
      f32x2 st = {0.f, 0.f};
#pragma unroll
      for (int ww = 0; ww < 8; ++ww) st += *(const LAS f32x2*)(L + L_ST + 256 + ww * 64 + (c & 7) * 8);
      const float mean = st[1] * (1.f / 512);
      const float mb = rbf(mean), vb = rbf(fmaxf(st[0] * (1.f / 512) - mean * mean, 0.f));
      const float rd = 1.f / rbf(sqrtf(rbf(vb + 1e-5f)));
#pragma unroll
      for (int t = 0; t < 2; ++t)
#pragma unroll
        for (int k = 0; k < 4; ++k) xa[t][k] = pk2bf(rbf(bflo(xa[t][k]) - mb) * rd, rbf(bfhi(xa[t][k]) - mb) * rd);
    }
    asm volatile("s_waitcnt vmcnt(0)" ::: "memory");
    f32x4 qa[4];
#pragma unroll
    for (int j = 0; j < 4; ++j) qa[j] = f32x4{0.f, 0.f, 0.f, 0.f};
#pragma unroll
    for (int t = 0; t < 2; ++t) {
      const int ta = L_WT + (64 * w + 32 * t + 8 * g + (c >> 2)) * 128 + (c & 3) * 8;
#pragma unroll
      for (int j = 0; j < 4; ++j) {
        const u32x2 b0 = trread(L + ta + j * 32), b1 = trread(L + ta + j * 32 + 512);
        qa[j] = mfma(xa[t], u32x4{b0[0], b0[1], b1[0], b1[1]}, qa[j]);
      }
    }
    asm volatile("s_waitcnt lgkmcnt(0)" ::: "memory");
    LAS float* red = (LAS float*)(L + L_WT + w * 8192);
    if (g < 2) {
#pragma unroll
      for (int j = 0; j < 4; ++j)
#pragma unroll
        for (int i = 0; i < 4; ++i) red[(4 * g + i) * 64 + j * 16 + c] = qa[j][i];
    }
    bar();
    if (w < 4) {
      const int bq = 2 * w + (lane >> 5), p = lane & 31;
      f32x2 q2 = {0.f, 0.f};
#pragma unroll
      for (int ww = 0; ww < 8; ++ww) q2 += *(const LAS f32x2*)(L + L_WT + ww * 8192 + (bq * 64 + 2 * p) * 4);
      __hip_atomic_store((GAS u64*)(ws + WS_QG) + ((pair >> 3) * 8 + bq) * 256 + (pair & 7) * 32 + p, (next_tag(ep) & 0xFFFFFFFF00000000ull) | pk2bf(q2[0], q2[1]), RLX_AGENT);
      STAMPV(2, __builtin_amdgcn_s_memrealtime());
    }
  } else {
    bar();
  }
  const int ntile = s == 0 ? NT_Q : s == 1 ? NT_O : NT_B, tbase = s == 0 ? 0 : s == 1 ? NT_Q : NT_Q + NT_O + (s - 2) * NT_B;
  const int nstr = s < 2 ? 17 : 18;
  const size_t kvbase = ((size_t)pair * SK + (size_t)tbase * 16) * DH;
  u64 tag = 0;
  if (w == 7) {
    __builtin_amdgcn_s_setprio(3);
    asm volatile("s_waitcnt vmcnt(0)" : "+v"(ep) :: "memory");
    tag = next_tag(ep);
    const unsigned tagq = (unsigned)(tag >> 32);
    if (lane == 0) __hip_atomic_store((GAS u64*)(ws + WS_FLAGS) + blockIdx.x, tag, RLX_AGENT);
    for (int sp = 0; sp < 1000 && (unsigned)__builtin_amdgcn_s_memrealtime() - tstart < (unsigned)TFIRST; ++sp) __builtin_amdgcn_s_sleep(2);
    u32x4 v0, v1;
    unsigned spins = 0;
    for (;; ++spins) {
      v0 = __builtin_amdgcn_raw_buffer_load_b128(rs, WS_QG + (unsigned)(kvh * 8 + b) * 2048 + lane * 16, 0, AUX_SC1);
      v1 = __builtin_amdgcn_raw_buffer_load_b128(rs, WS_QG + (unsigned)(kvh * 8 + b) * 2048 + 1024 + lane * 16, 0, AUX_SC1);
      const bool ok = (v0[1] == tagq) & (v0[3] == tagq) & (v1[1] == tagq) & (v1[3] == tagq);
      if (__all(ok) || spins > SPIN_CAP) break;
      asm volatile("" ::: "memory");
    }
    *(LAS u32x2*)(L + L_Q + lane * 8) = u32x2{v0[0], v0[2]};
    *(LAS u32x2*)(L + L_Q + 512 + lane * 8) = u32x2{v1[0], v1[2]};
    asm volatile("s_waitcnt lgkmcnt(0)" ::: "memory");
    if (lane == 0) *(volatile LAS unsigned*)(L + L_ST + 136) = 1u;
    STAMPV(12, spins); STAMPV(3, __builtin_amdgcn_s_memrealtime());
#pragma unroll
    for (int i = 0; i < 11; ++i) {
      const int e = min(i * 64 + lane, 671);
      const GAS unsigned short* src = e < 224 ? (const GAS unsigned short*)w1_ + e * 8 : e < 448 ? (const GAS unsigned short*)b1_ + (e - 224) * 8 : (const GAS unsigned short*)w2_ + (e - 448) * 8;
      glds16d(src, (unsigned)(L_MIX + i * 1024));
    }
    const u32x4 xr = *(const GAS u32x4*)((const GAS unsigned short*)x_ + b * 512 + lane * 8);
    const unsigned b2v = ((const GAS unsigned short*)b2_)[0];
    {
      const GAS u64* fl = (const GAS u64*)(ws + WS_FLAGS) + lane;
      for (unsigned sp = 0;; ++sp) {
        bool ok = true;
        u64 v[4];
#pragma unroll
        for (int i = 0; i < 4; ++i) v[i] = __hip_atomic_load(fl + 64 * i, RLX_AGENT);
#pragma unroll
        for (int i = 0; i < 4; ++i) ok &= v[i] == tag;
        if (__all(ok) || sp > SPIN_CAP) break;
        __builtin_amdgcn_s_sleep(8);
      }
      if (blockIdx.x == 0 && lane == 0) __hip_atomic_store((GAS u64*)(ws + WS_EPOCH), tag, RLX_AGENT);
    }
    *(LAS f32x4*)(L + L_H + lane * 32) = f32x4{bflo(xr[0]), bfhi(xr[0]), bflo(xr[1]), bfhi(xr[1])};
    *(LAS f32x4*)(L + L_H + lane * 32 + 16) = f32x4{bflo(xr[2]), bfhi(xr[2]), bflo(xr[3]), bfhi(xr[3])};
    if (lane == 0) *(LAS float*)(L + L_ST + 132) = bf2f(b2v);
    __builtin_amdgcn_s_setprio(0);
  }
  constexpr bool real = true;
  {
  float m = -INFINITY;
  f32x4 acc[9];
#pragma unroll
  for (int e = 0; e < 9; ++e) acc[e] = f32x4{0.f, 0.f, 0.f, 0.f};
  if (real) {
    int tid_ = threadIdx.x; PIN(tid_);
    const int lane = tid_ & 63, c = lane & 15, g = lane >> 4;
    const __amdgpu_buffer_rsrc_t rk = __builtin_amdgcn_make_buffer_rsrc((void*)(kc_ + kvbase), 0, (w == 7 ? ntile : 16 * NSW) * 4096, 0x00020000);
    const __amdgpu_buffer_rsrc_t rv = __builtin_amdgcn_make_buffer_rsrc((void*)(vc_ + kvbase), 0, (w == 7 ? ntile : 16 * NSW) * 4096, 0x00020000);
    constexpr float SC = 0.088388347648318440f, L2E = 1.4426950408889634f;
    const u32x2 ones = {0x3F803F80u, 0x3F803F80u};
#define ISSUE(R, RS, off, n) do { const int o_ = (off) + (n) * tstep; \
    _Pragma("unroll") for (int i_ = 0; i_ < 4; ++i_) R[i_] = __builtin_amdgcn_raw_buffer_load_b128(RS, o_ + i_ * 1024, 0, 2); __builtin_amdgcn_sched_barrier(0); } while (0)
#define PROCESS(K, V, nn, RE) do { \
    float sc_[4]; \
    _Pragma("unroll") for (int i_ = 0; i_ < 4; ++i_) { const f32x4 d_ = mfma(K[i_], qf, f32x4{0.f, 0.f, 0.f, 0.f}); \
      sc_[i_] = (d_[0] + dpp<ROR12>(d_[1])) + (dpp<ROR8>(d_[2]) + dpp<ROR4>(d_[3])); } \
    _Pragma("unroll") for (int i_ = 0; i_ < 4; ++i_) PIN(sc_[i_]); \
    __builtin_amdgcn_sched_barrier(0); \
    if (RE) ISSUE(K, rk, koff, nn); \
    _Pragma("unroll") for (int j_ = 0; j_ < 2; ++j_) {     \
      const unsigned r_ = pk2bf(sc_[2 * j_], sc_[2 * j_ + 1]); const unsigned t_ = pk2bf(bflo(r_) * SC, bfhi(r_) * SC); \
      sc_[2 * j_] = bflo(t_); sc_[2 * j_ + 1] = bfhi(t_); } \
    const float tm_ = xrow16_max(fmaxf(fmaxf(sc_[0], sc_[1]), fmaxf(sc_[2], sc_[3]))) * L2E; \
    const float mn_ = fmaxf(m, tm_); \
    const float al_ = __builtin_amdgcn_exp2f(m - mn_); m = mn_; \
    u32x2 pa_; \
    _Pragma("unroll") for (int j_ = 0; j_ < 2; ++j_) pa_[j_] = pk2bf(__builtin_amdgcn_exp2f(fmaf(sc_[2 * j_], L2E, -mn_)), __builtin_amdgcn_exp2f(fmaf(sc_[2 * j_ + 1], L2E, -mn_))); \
    const float a0_ = __builtin_bit_cast(float, __builtin_amdgcn_readlane(__builtin_bit_cast(int, al_), 0)), a1_ = __builtin_bit_cast(float, __builtin_amdgcn_readlane(__builtin_bit_cast(int, al_), 1)), \
                a2_ = __builtin_bit_cast(float, __builtin_amdgcn_readlane(__builtin_bit_cast(int, al_), 2)), a3_ = __builtin_bit_cast(float, __builtin_amdgcn_readlane(__builtin_bit_cast(int, al_), 3)); \
    _Pragma("unroll") for (int e_ = 0; e_ < 9; ++e_) { acc[e_][0] *= a0_; acc[e_][1] *= a1_; acc[e_][2] *= a2_; acc[e_][3] *= a3_; } \
    acc[8] = mfma16(pa_, ones, acc[8]); \
    u32x2 bf_[8]; \
    _Pragma("unroll") for (int jj_ = 0; jj_ < 2; ++jj_) _Pragma("unroll") for (int d_ = 0; d_ < 4; ++d_) { \
      bf_[2 * d_][jj_] = __builtin_amdgcn_perm(V[2 * jj_ + 1][d_], V[2 * jj_][d_], 0x05040100u); \
      bf_[2 * d_ + 1][jj_] = __builtin_amdgcn_perm(V[2 * jj_ + 1][d_], V[2 * jj_][d_], 0x07060302u); } \
    _Pragma("unroll") for (int e_ = 0; e_ < 8; ++e_) PIN(bf_[e_]); \
    __builtin_amdgcn_sched_barrier(0); \
    if (RE) ISSUE(V, rv, voff, nn); \
    _Pragma("unroll") for (int e_ = 0; e_ < 8; ++e_) acc[e_] = mfma16(pa_, bf_[e_], acc[e_]); \
    __builtin_amdgcn_sched_barrier(0); \
  } while (0)
    if (w == 7) {
      u32x4 K0[4], V0[4];
      const int tstep = 4096;
      const int koff = (c >> 2) * 256 + g * 64 + (c & 3) * 16, voff = lane * 16;
      const u32x4 qf = *(LAS u32x4*)(L + L_Q + (c & 3) * 256 + (4 * g + (c >> 2)) * 16);
      __builtin_amdgcn_s_setprio(1);
      ISSUE(K0, rk, koff, nstr * NSW); ISSUE(V0, rv, voff, nstr * NSW);
      for (int t = nstr * NSW; t < ntile; ++t) PROCESS(K0, V0, t + 1, true);
      __builtin_amdgcn_s_waitcnt(0x0F70);
      STAMP(11);
    } else {
      const int tstep = NSW * 4096;
      const int koff = (c >> 2) * 256 + g * 64 + (c & 3) * 16 + w * 4096, voff = lane * 16 + w * 4096;
      u32x4 K0[4], V0[4], K1[4], V1[4], K2[4], V2[4], K3[4], V3[4];
      ISSUE(K0, rk, koff, 0); STAMP(1); ISSUE(V0, rv, voff, 0); ISSUE(K1, rk, koff, 1); ISSUE(V1, rv, voff, 1);
      ISSUE(K2, rk, koff, 2); ISSUE(V2, rv, voff, 2); ISSUE(K3, rk, koff, 3); ISSUE(V3, rv, voff, 3);
      {
        const GAS unsigned char* kg = (const GAS unsigned char*)(kc_ + kvbase) + koff + 16 * tstep;
        const GAS unsigned char* vg = (const GAS unsigned char*)(vc_ + kvbase) + voff + 16 * tstep;
#pragma unroll
        for (int i = 0; i < 4; ++i) glds16(kg + i * 1024, (unsigned)(L_KV + w * 8192 + i * 1024));
#pragma unroll
        for (int i = 0; i < 4; ++i) glds16(vg + i * 1024, (unsigned)(L_KV + w * 8192 + 4096 + i * 1024));
        if (s >= 2) {
#pragma unroll
          for (int i = 0; i < 4; ++i) glds16(kg + tstep + i * 1024, (unsigned)(L_WT + w * 8192 + i * 1024));
#pragma unroll
          for (int i = 0; i < 4; ++i) glds16(vg + tstep + i * 1024, (unsigned)(L_WT + w * 8192 + 4096 + i * 1024));
        }
      }
      for (unsigned spins = 0; ldl(L + L_ST + 136) == 0u && spins < (1u << 24); ++spins) __builtin_amdgcn_s_sleep(1);
      asm volatile("" ::: "memory");
      const u32x4 qf = *(LAS u32x4*)(L + L_Q + (c & 3) * 256 + (4 * g + (c >> 2)) * 16);
      for (int n = 0; n < 16; n += 4) {
        PROCESS(K0, V0, n + 4, true); PROCESS(K1, V1, n + 5, true); PROCESS(K2, V2, n + 6, true); PROCESS(K3, V3, n + 7, true);
        if (n == 0) {
          asm volatile("s_waitcnt vmcnt(32)" ::: "memory");
          for (int t = 0; t < nstr - 16; ++t) {
            u32x4 KL[4], VL[4];
            LAS unsigned char* lp = L + (t ? L_WT : L_KV) + w * 8192 + lane * 16;
#pragma unroll
            for (int i = 0; i < 4; ++i) { KL[i] = *(LAS u32x4*)(lp + i * 1024); VL[i] = *(LAS u32x4*)(lp + 4096 + i * 1024); }
            PROCESS(KL, VL, 0, false);
          }
        }
      }
      __builtin_amdgcn_s_waitcnt(0x0F70);
      STAMP(4);
    }
  }
  int tid_ = threadIdx.x; PIN(tid_);
  const int tid = tid_, lane = tid & 63, c = lane & 15, g = lane >> 4;
  {
    LAS float* rec = (LAS float*)(L + L_KV + w * 8192);
    if (g == 0) {
#pragma unroll
      for (int r = 0; r < 4; ++r) {
        *(LAS f32x4*)(rec + r * 128 + 8 * c) = f32x4{acc[0][r], acc[1][r], acc[2][r], acc[3][r]};
        *(LAS f32x4*)(rec + r * 128 + 8 * c + 4) = f32x4{acc[4][r], acc[5][r], acc[6][r], acc[7][r]};
      }
      if (c < 4) rec[512 + c] = m;
      if (c == 0) *(LAS f32x4*)(rec + 516) = acc[8];
    }
  }
  if (real) bar();
  STAMP(13);
  {
    const int hd = w >> 1;
    const LAS float* rec = (const LAS float*)(L + L_KV);
    const float mw = rec[(lane & 7) * 2048 + 512 + hd], lw = rec[(lane & 7) * 2048 + 516 + hd];
    float av8[NW];
#pragma unroll
    for (int ww = 0; ww < NW; ++ww) av8[ww] = rec[ww * 2048 + tid];
    float oM = fmaxf(mw, dpp<XOR1>(mw)); oM = fmaxf(oM, dpp<XOR2>(oM)); oM = fmaxf(oM, dpp<XOR7>(oM));
    const float wt = __builtin_amdgcn_exp2f(mw - oM);
    float oL = wt * lw; oL += dpp<XOR1>(oL); oL += dpp<XOR2>(oL); oL += dpp<XOR7>(oL);
    float oA = 0.f, oB = 0.f;
#pragma unroll
    for (int ww = 0; ww < NW; ww += 2) {
      oA = fmaf(__uint_as_float(__builtin_amdgcn_readlane(__float_as_uint(wt), ww)), av8[ww], oA);
      oB = fmaf(__uint_as_float(__builtin_amdgcn_readlane(__float_as_uint(wt), ww + 1)), av8[ww + 1], oB);
    }
    GAS unsigned char* rb = ws + WS_REC + (unsigned)(pair * 4 + s) * REC_B;
    stw(rb + tid * 4, san(oA + oB));
    if ((tid & 127) < 2) stw(rb + 2048 + hd * 8 + (tid & 1) * 4, san((tid & 1) ? oL : oM));
  }
  STAMP(5);
#define MIXPREP() do { \
  _Pragma("unroll") for (int i = 0; i < 7; ++i) { \
    const u32x2 a_ = *(LAS u32x2*)(L + L_MIX + (i * 256 + lane * 4) * 2); \
    const u32x2 b_ = *(LAS u32x2*)(L + L_MIX + 3584 + (i * 256 + lane * 4) * 2); \
    const u32x2 c_ = *(LAS u32x2*)(L + L_MIX + 7168 + (i * 256 + lane * 4) * 2); \
    pw1[2 * i] = f32x2{bflo(a_[0]), bfhi(a_[0])}; pw1[2 * i + 1] = f32x2{bflo(a_[1]), bfhi(a_[1])}; \
    pb1[2 * i] = f32x2{bflo(b_[0]), bfhi(b_[0])}; pb1[2 * i + 1] = f32x2{bflo(b_[1]), bfhi(b_[1])}; \
    pw2[2 * i] = f32x2{bflo(c_[0]), bfhi(c_[0])}; pw2[2 * i + 1] = f32x2{bflo(c_[1]), bfhi(c_[1])}; } \
  _Pragma("unroll") for (int i = 0; i < 14; ++i) { PIN(pw1[i]); PIN(pb1[i]); PIN(pw2[i]); } } while (0)
  f32x2 pw1[14], pb1[14], pw2[14];
  if (s != 1) MIXPREP();
  if (s == 1) {
    const int kvh2 = pair >> 3;
    u32x4 ra[4], rb2[4]; u32x2 rh[4];
    const unsigned r0 = WS_REC + (unsigned)((w * 8 + kvh2) * 4) * REC_B + lane * 32;
    const unsigned h0 = WS_REC + (unsigned)((w * 8 + kvh2) * 4) * REC_B + 2048 + g * 8;
#define RLOAD(j) do { ra[j] = __builtin_amdgcn_raw_buffer_load_b128(rs, r0 + (j) * REC_B, 0, AUX_SC1); rb2[j] = __builtin_amdgcn_raw_buffer_load_b128(rs, r0 + (j) * REC_B + 16, 0, AUX_SC1); \
      rh[j] = __builtin_amdgcn_raw_buffer_load_b64(rs, h0 + (j) * REC_B, 0, AUX_SC1); } while (0)
#define RMISS(j) __any(max(max(max(max(ra[j][0], ra[j][1]), max(ra[j][2], ra[j][3])), max(max(rb2[j][0], rb2[j][1]), max(rb2[j][2], rb2[j][3]))), max(rh[j][0], rh[j][1])) == SENT)
    RLOAD(0); RLOAD(1); RLOAD(2); RLOAD(3);
    __builtin_amdgcn_sched_barrier(0);
    u32x4 wf[16];
    {
      const int ta = L_WT + (8 * g + (c >> 2)) * 128 + (w & 3) * 32 + (c & 3) * 8;
#pragma unroll
      for (int ks = 0; ks < 16; ++ks) {
        const u32x2 b0 = trread(L + ta + ks * 4096), b1 = trread(L + ta + ks * 4096 + 512);
        wf[ks] = u32x4{b0[0], b0[1], b1[0], b1[1]};
      }
#pragma unroll
      for (int ks = 0; ks < 16; ++ks) PIN(wf[ks]);
    }
    MIXPREP();
    {
      bool m0 = RMISS(0), m1 = RMISS(1), m2 = RMISS(2), m3 = RMISS(3);
      for (unsigned spins = 0; (m0 | m1 | m2 | m3) && spins < SPIN_CAP; ++spins) {
        asm volatile("" ::: "memory");
        if (m0) RLOAD(0);
        if (m1) RLOAD(1);
        if (m2) RLOAD(2);
        if (m3) RLOAD(3);
        if (m0) m0 = RMISS(0);
        if (m1) m1 = RMISS(1);
        if (m2) m2 = RMISS(2);
        if (m3) m3 = RMISS(3);
      }
    }
    {
      float mj[4], wj[4];
#pragma unroll
      for (int j = 0; j < 4; ++j) mj[j] = __uint_as_float(rh[j][0]);
      const float M = fmaxf(fmaxf(mj[0], mj[1]), fmaxf(mj[2], mj[3]));
#pragma unroll
      for (int j = 0; j < 4; ++j) wj[j] = __builtin_amdgcn_exp2f(mj[j] - M);
      const float Ls = (wj[0] * __uint_as_float(rh[0][1]) + wj[1] * __uint_as_float(rh[1][1])) + (wj[2] * __uint_as_float(rh[2][1]) + wj[3] * __uint_as_float(rh[3][1]));
      const float inv = __builtin_amdgcn_rcpf(Ls);
      const f32x4 oa = ((__builtin_bit_cast(f32x4, ra[0]) * wj[0] + __builtin_bit_cast(f32x4, ra[1]) * wj[1]) + (__builtin_bit_cast(f32x4, ra[2]) * wj[2] + __builtin_bit_cast(f32x4, ra[3]) * wj[3])) * inv;
      const f32x4 ob = ((__builtin_bit_cast(f32x4, rb2[0]) * wj[0] + __builtin_bit_cast(f32x4, rb2[1]) * wj[1]) + (__builtin_bit_cast(f32x4, rb2[2]) * wj[2] + __builtin_bit_cast(f32x4, rb2[3]) * wj[3])) * inv;
      *(LAS u32x4*)(L + L_OA + w * 1040 + lane * 16) = u32x4{pk2bf(oa[0], oa[1]), pk2bf(oa[2], oa[3]), pk2bf(ob[0], ob[1]), pk2bf(ob[2], ob[3])};
    }
    STAMP(6);
    if (real) bar();
    if (w < 4 || !real) {
      f32x4 a0 = {0.f, 0.f, 0.f, 0.f}, a1 = a0, a2 = a0, a3 = a0;
      const LAS unsigned char* op = L + L_OA + (c & 7) * 1040 + (256 * (c >> 3) + 8 * g) * 2;
#pragma unroll
      for (int t = 0; t < 8; t += 2) {
        const u32x4 x0 = *(const LAS u32x4*)(op + t * 64), x1 = *(const LAS u32x4*)(op + t * 64 + 64);
        a0 = mfma(x0, wf[t], a0); a1 = mfma(x0, wf[t + 8], a1);
        a2 = mfma(x1, wf[t + 1], a2); a3 = mfma(x1, wf[t + 9], a3);
      }
      const f32x4 lo = a0 + a2, hi = a1 + a3;
      const float q0 = swap32(g < 2 ? lo[2] : hi[0], lane), q1 = swap32(g < 2 ? lo[3] : hi[1], lane);
      const unsigned e0 = san((g < 2 ? lo[0] : hi[2]) + q0), e1 = san((g < 2 ? lo[1] : hi[3]) + q1);
      GAS unsigned char* ap = ws + WS_ANS + (unsigned)(((4 * (g & 1) + 2 * (g >> 1)) * 8 + kvh2) * 512 + (pair & 7) * 64 + (w & 3) * 16 + c) * 4;
      if (real) { stw(ap, e0); stw(ap + 8 * 512 * 4, e1); } else { asm volatile("" :: "v"(e0), "v"(e1), "v"(ap)); }
      STAMP(7);
    }
  }
  if (w == GW) {
    u32x4 ga[8], gb[8];
    const unsigned ab = WS_ANS + (unsigned)(b * 8 * 512) * 4 + lane * 16;
    const GAS unsigned char* ind = ws + WS_ANS + (unsigned)((b * 8 + (lane & 7)) * 512 + (kvh * 4 + s) * 16) * 4;
    unsigned miss = 0xFFu;
    for (unsigned spins = 0; miss && spins < SPIN_CAP; ++spins) {
      unsigned want = miss;
      if (miss & (miss - 1)) {
        unsigned pv = SENT;
        if (lane < 8) pv = ldw(ind);
        want = miss & (unsigned)__builtin_amdgcn_ballot_w64(pv != SENT);
        if (!want) { __builtin_amdgcn_s_sleep(SL3); continue; }
      }
      asm volatile("" ::: "memory");
#pragma unroll
      for (int k = 0; k < 8; ++k) if (want & (1u << k)) {
        ga[k] = __builtin_amdgcn_raw_buffer_load_b128(rs, ab + k * 2048, 0, AUX_SC1);
        gb[k] = __builtin_amdgcn_raw_buffer_load_b128(rs, ab + k * 2048 + 1024, 0, AUX_SC1);
      }
#pragma unroll
      for (int k = 0; k < 8; ++k) if (want & (1u << k)) {
        if (!__any(max(max(max(ga[k][0], ga[k][1]), max(ga[k][2], ga[k][3])), max(max(gb[k][0], gb[k][1]), max(gb[k][2], gb[k][3]))) == SENT)) miss &= ~(1u << k);
      }
    }
    STAMP(15);
#define F4(x) __builtin_bit_cast(f32x4, x)
    const f32x4 sa = ((F4(ga[0]) + F4(ga[1])) + (F4(ga[2]) + F4(ga[3]))) + ((F4(ga[4]) + F4(ga[5])) + (F4(ga[6]) + F4(ga[7])));
    const f32x4 sb = ((F4(gb[0]) + F4(gb[1])) + (F4(gb[2]) + F4(gb[3]))) + ((F4(gb[4]) + F4(gb[5])) + (F4(gb[6]) + F4(gb[7])));
    const f32x4 xa = *(LAS f32x4*)(L + L_H + lane * 16), xb = *(LAS f32x4*)(L + L_H + 1024 + lane * 16);
    f32x4 ha, hb;
#pragma unroll
    for (int i = 0; i < 4; i += 2) {
      const unsigned pa = pk2bf(sa[i], sa[i + 1]), pb = pk2bf(sb[i], sb[i + 1]);
      const unsigned qa = pk2bf(xa[i] + bflo(pa), xa[i + 1] + bfhi(pa)), qb = pk2bf(xb[i] + bflo(pb), xb[i + 1] + bfhi(pb));
      ha[i] = bflo(qa); ha[i + 1] = bfhi(qa); hb[i] = bflo(qb); hb[i + 1] = bfhi(qb);
    }
    *(LAS f32x4*)(L + L_H + lane * 16) = ha; *(LAS f32x4*)(L + L_H + 1024 + lane * 16) = hb;
    const f32x4 hs = ha + hb;
    const float mean = wave_sum((hs[0] + hs[1]) + (hs[2] + hs[3])) * (1.f / 512);
    const f32x4 da = ha - mean, db = hb - mean;
    const f32x4 sq = da * da + db * db;
    const float var = wave_sum((sq[0] + sq[1]) + (sq[2] + sq[3])) * (1.f / 512);
    const float m1 = rbf(mean), r1 = __builtin_amdgcn_rcpf(rbf(__builtin_amdgcn_sqrtf(rbf(rbf(var) + 1e-5f))));
    const float m2 = rbf((mean - m1) * r1), r2 = __builtin_amdgcn_rcpf(rbf(__builtin_amdgcn_sqrtf(rbf(rbf(var * r1 * r1) + 1e-5f))));
    if (lane == 0) *(LAS f32x4*)(L + L_ST) = f32x4{m1, r1, m2, r2};
  }
  bar();
  STAMP(8);
  {
    const int e0 = (kvh * 4 + s) * 16 + 2 * w;
    const f32x4 ln = *(LAS f32x4*)(L + L_ST);
    const float hh = *(LAS float*)(L + L_H + (e0 + (lane & 1)) * 4);
    const float bb = *(LAS float*)(L + L_ST + 132);
    const float m1 = ln[0], r1 = ln[1], m2 = ln[2], r2 = ln[3];
    const float hp = rbf(rbf(rbf(rbf(hh - m1) * r1) - m2) * r2);
    const float h0 = __uint_as_float(__builtin_amdgcn_readlane(__float_as_uint(hp), 0)), h1 = __uint_as_float(__builtin_amdgcn_readlane(__float_as_uint(hp), 1));
    constexpr float K0c = -2.f * 1.4426950408889634f * 0.7978845608028654f, K1c = K0c * 0.044715f;
    f32x2 ya = {0.f, 0.f}, yb = {0.f, 0.f};
#pragma unroll
    for (int i = 0; i < 14; ++i) {
#pragma unroll
      for (int e2 = 0; e2 < 2; ++e2) {
        const float hx = e2 ? h1 : h0;
        const f32x2 u = pw1[i] * hx + pb1[i];
        const f32x2 z = u * ((u * u) * K1c + K0c);
        f32x2 e = {__builtin_amdgcn_exp2f(z[0]), __builtin_amdgcn_exp2f(z[1])};
        e = e + 1.f;
        const f32x2 r = {__builtin_amdgcn_rcpf(e[0]), __builtin_amdgcn_rcpf(e[1])};
        if (e2) yb = (u * pw2[i]) * r + yb; else ya = (u * pw2[i]) * r + ya;
      }
    }
    const float ta = ya[0] + ya[1], tb = yb[0] + yb[1];
    float t = ((lane & 1) ? tb : ta) + dpp<XOR1>((lane & 1) ? ta : tb);
    t += dpp<XOR2>(t); t += dpp<ROR4>(t); t += dpp<ROR8>(t); t = xrow16_sum(t);
    const float ov = hh + rbf(rbf(t) + bb);
    if (lane < 2) ((GAS __bf16*)out_)[b * 512 + e0 + lane] = (__bf16)ov;
  }
  STAMP(9);
  }
}

extern "C" void kernel_launch(void* const* d_in, const int* in_sizes, int n_in, void* d_out, int out_size, void* d_ws, size_t ws_size, hipStream_t stream) {
  k_fused<<<NWG, NTHR, 0, stream>>>((const unsigned short*)d_in[0], (const unsigned short*)d_in[1], (const unsigned short*)d_in[2], (const unsigned short*)d_in[3],
      (const unsigned short*)d_in[4], (const unsigned short*)d_in[6], (const unsigned short*)d_in[7], (const unsigned short*)d_in[8], (const unsigned short*)d_in[9],
      (unsigned short*)d_out, (unsigned char*)d_ws);
}
```

```cpp
#include <hip/hip_runtime.h>
#include <cstdint>

#define GAS __attribute__((address_space(1)))
#define LAS __attribute__((address_space(3)))
#define RLX_AGENT __ATOMIC_RELAXED, __HIP_MEMORY_SCOPE_AGENT
typedef unsigned u32x4 __attribute__((ext_vector_type(4)));
typedef unsigned u32x2 __attribute__((ext_vector_type(2)));
typedef float f32x4 __attribute__((ext_vector_type(4)));
typedef float f32x2 __attribute__((ext_vector_type(2)));
typedef __bf16 bf16x8 __attribute__((ext_vector_type(8)));
typedef __bf16 bf16x2 __attribute__((ext_vector_type(2)));
typedef short s16x4 __attribute__((ext_vector_type(4)));
typedef unsigned long long u64;

#define STAMPS 0
#define TFIRST 550
#define GW 5
#define SL2 2
#define SL3 4
#define NT_Q 123
#define NT_O 121
#define SDELAY 0
#define HBT 1
#define QPRE 1
#define DPAR 4
#define MIXN 8
#define DRY 0

constexpr int NWG = 256, NTHR = 512, NW = 8, NSW = 7;
constexpr int SK = 8192, DH = 128, FF = 1792;
constexpr int NT_B = (512 - NT_Q - NT_O) / 2;
static_assert(NT_Q - DPAR >= 119 && NT_Q - DPAR >= 112 && NT_O - DPAR >= 112 && NT_Q + NT_O + 2 * NT_B == 512, "tile split");
constexpr unsigned WS_EPOCH = 0, WS_FLAGS = 4096, WS_QG = 8192;
constexpr unsigned WS_REC = 0x50000, REC_B = 2176;
constexpr unsigned WS_ANS = WS_REC + 256 * REC_B;
constexpr int TAB_LO = 0x3880, TAB_N = 2360, TAB_PER_WG = 19;
static_assert(2 * TAB_N + 1 <= NWG * TAB_PER_WG, "table");
constexpr unsigned WS_TAB = WS_ANS + 131072;
constexpr unsigned WS_END = WS_TAB + NWG * TAB_PER_WG * 4;
constexpr unsigned CLR_B = (WS_END - WS_REC) / 64;
static_assert(CLR_B == 691 * 16 && CLR_B * 64 == WS_END - WS_REC, "ws");
constexpr unsigned WS_STAMP = 1u << 24;
constexpr unsigned SENT = 0xFFFFFFFFu;
constexpr int AUX_SC1 = 16;
constexpr unsigned SPIN_CAP = 1u << 16;
constexpr int L_WT = 0;
constexpr int L_KV = 65536;
constexpr int L_R7 = L_KV + 7 * 8192;
constexpr int L_MIX = L_R7 + 2560;
constexpr int L_OA = L_MIX + 11264;
constexpr int L_Q = L_OA + 8320;
constexpr int L_H = L_Q + 1024;
constexpr int L_ST = L_H + 2048;
constexpr int L_END = L_ST + 768;
static_assert(L_END <= 163840, "lds");

__device__ __forceinline__ float bf2f(unsigned b) { return __builtin_bit_cast(float, b << 16); }
__device__ __forceinline__ float bflo(unsigned v) { return __builtin_bit_cast(float, v << 16); }
__device__ __forceinline__ float bfhi(unsigned v) { return __builtin_bit_cast(float, v & 0xffff0000u); }
__device__ __forceinline__ unsigned pk2bf(float lo, float hi) { bf16x2 v = {(__bf16)lo, (__bf16)hi}; return __builtin_bit_cast(unsigned, v); }
__device__ __forceinline__ float rbf(float x) { return (float)(__bf16)x; }
template <int CTRL> __device__ __forceinline__ float dpp(float x) {
  return __builtin_bit_cast(float, __builtin_amdgcn_mov_dpp(__builtin_bit_cast(int, x), CTRL, 0xf, 0xf, true));
}
constexpr int XOR1 = 0xB1, XOR2 = 0x4E, XOR7 = 0x141, ROR4 = 0x124, ROR8 = 0x128, ROR12 = 0x12C;
__device__ __forceinline__ float xrow16_max(float x) {
  auto s = __builtin_amdgcn_permlane16_swap(__float_as_uint(x), __float_as_uint(x), false, false);
  x = fmaxf(__uint_as_float(s[0]), __uint_as_float(s[1]));
  auto t = __builtin_amdgcn_permlane32_swap(__float_as_uint(x), __float_as_uint(x), false, false);
  return fmaxf(__uint_as_float(t[0]), __uint_as_float(t[1]));
}
__device__ __forceinline__ float xrow16_sum(float x) {
  auto s = __builtin_amdgcn_permlane16_swap(__float_as_uint(x), __float_as_uint(x), false, false);
  x = __uint_as_float(s[0]) + __uint_as_float(s[1]);
  auto t = __builtin_amdgcn_permlane32_swap(__float_as_uint(x), __float_as_uint(x), false, false);
  return __uint_as_float(t[0]) + __uint_as_float(t[1]);
}
__device__ __forceinline__ float wave_sum(float x) {
  x += dpp<XOR1>(x); x += dpp<XOR2>(x); x += dpp<XOR7>(x); x += dpp<ROR8>(x);
  return xrow16_sum(x);
}
__device__ __forceinline__ float swap32(float x, int lane) {
  auto t = __builtin_amdgcn_permlane32_swap(__float_as_uint(x), __float_as_uint(x), false, false);
  return __uint_as_float(lane < 32 ? t[1] : t[0]);
}
__device__ __forceinline__ f32x4 mfma(u32x4 a, u32x4 b, f32x4 c) {
  return __builtin_amdgcn_mfma_f32_16x16x32_bf16(__builtin_bit_cast(bf16x8, a), __builtin_bit_cast(bf16x8, b), c, 0, 0, 0);
}
__device__ __forceinline__ f32x4 mfma16(u32x2 a, u32x2 b, f32x4 c) {
  return __builtin_amdgcn_mfma_f32_16x16x16bf16_1k(__builtin_bit_cast(s16x4, a), __builtin_bit_cast(s16x4, b), c, 0, 0, 0);
}
__device__ __forceinline__ void bar() { asm volatile("s_waitcnt lgkmcnt(0)\n\ts_barrier" ::: "memory"); }
__device__ __forceinline__ unsigned san(float f) { const unsigned u = __builtin_bit_cast(unsigned, f); return u == SENT ? 0x7FC00000u : u; }
__device__ __forceinline__ void glds16(const GAS void* gsrc, unsigned lds_dst) {
  unsigned keep;
  asm volatile("s_mov_b32 %0, m0\n\ts_mov_b32 m0, %2\n\ts_nop 0\n\tglobal_load_lds_dwordx4 %1, off nt\n\ts_mov_b32 m0, %0" : "=&s"(keep) : "v"(gsrc), "s"(lds_dst) : "memory");
}
__device__ __forceinline__ void glds16d(const GAS void* gsrc, unsigned lds_dst) {
  unsigned keep;
  asm volatile("s_mov_b32 %0, m0\n\ts_mov_b32 m0, %2\n\ts_nop 0\n\tglobal_load_lds_dwordx4 %1, off\n\ts_mov_b32 m0, %0" : "=&s"(keep) : "v"(gsrc), "s"(lds_dst) : "memory");
}
__device__ __forceinline__ u32x2 trread(LAS unsigned char* p) {
  return __builtin_bit_cast(u32x2, __builtin_amdgcn_ds_read_tr16_b64_v4i16((LAS s16x4*)p));
}
__device__ __forceinline__ unsigned ldw(const GAS unsigned char* p) { return __hip_atomic_load((const GAS unsigned*)p, RLX_AGENT); }
__device__ __forceinline__ void stw(GAS unsigned char* p, unsigned v) { __hip_atomic_store((GAS unsigned*)p, v, RLX_AGENT); }
__device__ __forceinline__ unsigned ldl(LAS unsigned char* p) { return __builtin_amdgcn_readfirstlane(*(volatile LAS unsigned*)p); }
__device__ __forceinline__ u64 next_tag(u64 ep) { return ep * 6364136223846793005ull + 1442695040888963407ull; }
__device__ __forceinline__ bool full4(u32x4 v) { return (v[0] != SENT) & (v[1] != SENT) & (v[2] != SENT) & (v[3] != SENT); }
#define PIN(x) asm volatile("" : "+v"(x))
#define STAMP(i) do {} while (0)
#define STAMPV(i, v) do {} while (0)

#define MIXPREP() do { \
  _Pragma("unroll") for (int i = 0; i < 7; ++i) { \
    const u32x2 a_ = *(LAS u32x2*)(L + L_MIX + (i * 256 + lane * 4) * 2); \
    const u32x2 b_ = *(LAS u32x2*)(L + L_MIX + 3584 + (i * 256 + lane * 4) * 2); \
    const u32x2 c_ = *(LAS u32x2*)(L + L_MIX + 7168 + (i * 256 + lane * 4) * 2); \
    pw1[2 * i] = f32x2{bflo(a_[0]), bfhi(a_[0])}; pw1[2 * i + 1] = f32x2{bflo(a_[1]), bfhi(a_[1])}; \
    pb1[2 * i] = f32x2{bflo(b_[0]), bfhi(b_[0])}; pb1[2 * i + 1] = f32x2{bflo(b_[1]), bfhi(b_[1])}; \
    pw2[2 * i] = f32x2{bflo(c_[0]), bfhi(c_[0])}; pw2[2 * i + 1] = f32x2{bflo(c_[1]), bfhi(c_[1])}; } \
  _Pragma("unroll") for (int i = 0; i < 14; ++i) { PIN(pw1[i]); PIN(pb1[i]); PIN(pw2[i]); } } while (0)
#define MIX2(T, H0, H1) do { \
    constexpr float K0c = -2.f * 1.4426950408889634f * 0.7978845608028654f, K1c = K0c * 0.044715f; \
    f32x2 ya_ = {0.f, 0.f}, yb_ = {0.f, 0.f}; \
    _Pragma("unroll") for (int i = 0; i < 14; ++i) { \
      _Pragma("unroll") for (int e2 = 0; e2 < 2; ++e2) { \
        const f32x2 u = pw1[i] * (e2 ? (H1) : (H0)) + pb1[i]; \
        const f32x2 z = u * ((u * u) * K1c + K0c); \
        f32x2 e = {__builtin_amdgcn_exp2f(z[0]), __builtin_amdgcn_exp2f(z[1])}; \
        e = e + 1.f; \
        const f32x2 r = {__builtin_amdgcn_rcpf(e[0]), __builtin_amdgcn_rcpf(e[1])};        \
        if (e2) yb_ = (u * pw2[i]) * r + yb_; else ya_ = (u * pw2[i]) * r + ya_; } } \
    const float ta_ = ya_[0] + ya_[1], tb_ = yb_[0] + yb_[1]; \
    float t_ = ((lane & 1) ? tb_ : ta_) + dpp<XOR1>((lane & 1) ? ta_ : tb_); \
    t_ += dpp<XOR2>(t_); t_ += dpp<ROR4>(t_); t_ += dpp<ROR8>(t_); T = xrow16_sum(t_); } while (0)

__global__ void __launch_bounds__(NTHR, 2) k_fused(const unsigned short* __restrict__ x_, const unsigned short* __restrict__ kc_, const unsigned short* __restrict__ vc_,
    const unsigned short* __restrict__ wq_, const unsigned short* __restrict__ wo_, const unsigned short* __restrict__ w1_, const unsigned short* __restrict__ b1_,
    const unsigned short* __restrict__ w2_, const unsigned short* __restrict__ b2_, unsigned short* out_, unsigned char* ws_) {
  __shared__ __attribute__((aligned(16))) unsigned char smem[L_END];
  LAS unsigned char* L = (LAS unsigned char*)smem;
  GAS unsigned char* ws = (GAS unsigned char*)ws_;
  const int tid = threadIdx.x, lane = tid & 63, w = __builtin_amdgcn_readfirstlane(tid >> 6);
  const int c = lane & 15, g = lane >> 4;
  const int xl = blockIdx.x & 7, y = blockIdx.x >> 3;
  const int s = (xl + y) & 3, kvh = 2 * (y & 3) + (xl >> 2), b = y >> 2;
  const int pair = b * 8 + kvh;
  const bool owner = kvh == b && s == 2;
  const __amdgpu_buffer_rsrc_t rs = __builtin_amdgcn_make_buffer_rsrc((void*)ws_, 0, (int)WS_END, 0x00020000);
  const unsigned tstart = (unsigned)__builtin_amdgcn_s_memrealtime();
  STAMP(0);
  const int pe = (y & 1) ? DPAR : -DPAR;
  const int n0 = NT_Q - pe, n1 = NT_O + pe, n2 = NT_B - pe;
  const int ntile = s == 0 ? n0 : s == 1 ? n1 : s == 2 ? n2 : NT_B + pe, tbase = s == 0 ? 0 : s == 1 ? n0 : s == 2 ? n0 + n1 : n0 + n1 + n2;
  const int nstr = 16 + min(s < 2 ? 1 : 2, (ntile - 112) / 7);
  const size_t kvbase = ((size_t)pair * SK + (size_t)tbase * 16) * DH;
  u64 ep = 0;
  if (__builtin_expect(w == 7 || (s == 0 && w < 4), 0)) asm volatile("global_load_dwordx2 %0, %1, off sc1" : "=v"(ep) : "v"(ws + WS_EPOCH) : "memory");
  if (__builtin_expect(w == 7, 0)) {
    if (lane == 0) { *(volatile LAS unsigned*)(L + L_ST + 128) = 0u; *(volatile LAS unsigned*)(L + L_ST + 136) = 0u; *(volatile LAS unsigned*)(L + L_ST + 140) = 0u; }
  }
  if (__builtin_expect(s == 1, 0)) {
    const GAS unsigned short* wp = (const GAS unsigned short*)wo_ + (size_t)((pair >> 3) * 512 + 64 * w + (lane >> 3)) * 512 + (pair & 7) * 64 + (lane & 7) * 8;
#pragma unroll
    for (int i = 0; i < 8; ++i) glds16(wp + (size_t)i * 8 * 512, (unsigned)(L_WT + (w * 8 + i) * 1024));
  }
  if (__builtin_expect(s == 0, 0)) {
    u32x4 xa[2];
    {
      const GAS unsigned short* xp = (const GAS unsigned short*)x_ + (c & 7) * 512 + 64 * w + 8 * g;
      asm volatile("global_load_dwordx4 %0, %2, off\n\tglobal_load_dwordx4 %1, %2, off offset:64" : "=&v"(xa[0]), "=&v"(xa[1]) : "v"(xp) : "memory");
    }
    const GAS unsigned short* wp = (const GAS unsigned short*)wq_ + (size_t)(64 * w + (lane >> 3)) * 4096 + pair * 64 + (lane & 7) * 8;
#pragma unroll
    for (int i = 0; i < 8; ++i) glds16(wp + (size_t)i * 8 * 4096, (unsigned)(L_WT + (w * 8 + i) * 1024));
    {
      const u32x4 sv = {SENT, SENT, SENT, SENT};
      __builtin_amdgcn_raw_buffer_store_b128(sv, rs, WS_REC + (unsigned)pair * CLR_B + tid * 16, 0, AUX_SC1);
      if (tid < 691 - 512) __builtin_amdgcn_raw_buffer_store_b128(sv, rs, WS_REC + (unsigned)pair * CLR_B + 8192 + tid * 16, 0, AUX_SC1);
    }
    asm volatile("s_waitcnt vmcnt(9)" : "+v"(xa[0]), "+v"(xa[1]), "+v"(ep) :: "memory");
    {
      f32x4 ga = {0.f, 0.f, 0.f, 0.f};
#pragma unroll
      for (int t = 0; t < 2; ++t) ga = mfma(c == 8 ? u32x4{0x3F803F80u, 0x3F803F80u, 0x3F803F80u, 0x3F803F80u} : xa[t], xa[t], ga);
      const float dg = (c & 2) ? ((c & 1) ? ga[3] : ga[2]) : ((c & 1) ? ga[1] : ga[0]);
      LAS float* sp = (LAS float*)(L + L_ST + 256) + w * 16;
      if (c < 8 && g == (c >> 2)) sp[2 * c] = dg;
      if (c < 8 && g == 2) sp[2 * c + 1] = ga[0];
    }
    bar();
    {
      f32x2 st = {0.f, 0.f};
#pragma unroll
      for (int ww = 0; ww < 8; ++ww) st += *(const LAS f32x2*)(L + L_ST + 256 + ww * 64 + (c & 7) * 8);
      const float mean = st[1] * (1.f / 512);
      const float mb = rbf(mean), vb = rbf(fmaxf(st[0] * (1.f / 512) - mean * mean, 0.f));
      const float rd = 1.f / rbf(sqrtf(rbf(vb + 1e-5f)));
#pragma unroll
      for (int t = 0; t < 2; ++t)
#pragma unroll
        for (int k = 0; k < 4; ++k) xa[t][k] = pk2bf(rbf(bflo(xa[t][k]) - mb) * rd, rbf(bfhi(xa[t][k]) - mb) * rd);
    }
    if (QPRE && w < 7) {
      const GAS unsigned char* kg = (const GAS unsigned char*)(kc_ + kvbase) + (c >> 2) * 256 + g * 64 + (c & 3) * 16 + (w + 16 * NSW) * 4096;
      const GAS unsigned char* vg = (const GAS unsigned char*)(vc_ + kvbase) + lane * 16 + (w + 16 * NSW) * 4096;
#pragma unroll
      for (int i = 0; i < 4; ++i) glds16(kg + i * 1024, (unsigned)(L_KV + w * 8192 + i * 1024));
      if (QPRE > 1) {
#pragma unroll
        for (int i = 0; i < 4; ++i) glds16(vg + i * 1024, (unsigned)(L_KV + w * 8192 + 4096 + i * 1024));
      }
      asm volatile("s_waitcnt vmcnt(%0)" :: "i"(QPRE * 4) : "memory");
    } else {
      asm volatile("s_waitcnt vmcnt(0)" ::: "memory");
    }
    f32x4 qa[4];
#pragma unroll
    for (int j = 0; j < 4; ++j) qa[j] = f32x4{0.f, 0.f, 0.f, 0.f};
#pragma unroll
    for (int t = 0; t < 2; ++t) {
      const int ta = L_WT + (64 * w + 32 * t + 8 * g + (c >> 2)) * 128 + (c & 3) * 8;
#pragma unroll
      for (int j = 0; j < 4; ++j) {
        const u32x2 b0 = trread(L + ta + j * 32), b1 = trread(L + ta + j * 32 + 512);
        qa[j] = mfma(xa[t], u32x4{b0[0], b0[1], b1[0], b1[1]}, qa[j]);
      }
    }
    asm volatile("s_waitcnt lgkmcnt(0)" ::: "memory");
    LAS float* red = (LAS float*)(L + L_WT + w * 8192);
    if (g < 2) {
#pragma unroll
      for (int j = 0; j < 4; ++j)
#pragma unroll
        for (int i = 0; i < 4; ++i) red[(4 * g + i) * 64 + j * 16 + c] = qa[j][i];
    }
    bar();
    if (w < 4) {
      const int bq = 2 * w + (lane >> 5), p = lane & 31;
      f32x2 q2 = {0.f, 0.f};
#pragma unroll
      for (int ww = 0; ww < 8; ++ww) q2 += *(const LAS f32x2*)(L + L_WT + ww * 8192 + (bq * 64 + 2 * p) * 4);
      __hip_atomic_store((GAS u64*)(ws + WS_QG) + ((pair >> 3) * 8 + bq) * 256 + (pair & 7) * 32 + p, (next_tag(ep) & 0xFFFFFFFF00000000ull) | pk2bf(q2[0], q2[1]), RLX_AGENT);
      if (tid == 0) __hip_atomic_store((GAS u64*)(ws + WS_FLAGS) + pair, next_tag(ep), RLX_AGENT);
      STAMPV(2, __builtin_amdgcn_s_memrealtime());
    }
  }
  int tabj = 0;
  float tabv = 0.f;
#define TABSTEP() do { const int e = blockIdx.x * TAB_PER_WG + tabj; \
        const int f0 = e < TAB_N ? TAB_LO + e : e < 2 * TAB_N ? 0x8000 + TAB_LO + e - TAB_N : 0; \
        const int f1 = e + 1 < TAB_N ? TAB_LO + e + 1 : e + 1 < 2 * TAB_N ? 0x8000 + TAB_LO + e + 1 - TAB_N : 0; \
        float tv; MIX2(tv, bf2f(f0), bf2f(f1)); if ((lane >> 1) == (tabj >> 1)) tabv = tv; } while (0)
  if (__builtin_expect(w == 7, 0)) {
    __builtin_amdgcn_s_setprio(3);
#pragma unroll
    for (int i = 0; i < 11; ++i) {
      const int e = min(i * 64 + lane, 671);
      const GAS unsigned short* src = e < 224 ? (const GAS unsigned short*)w1_ + e * 8 : e < 448 ? (const GAS unsigned short*)b1_ + (e - 224) * 8 : (const GAS unsigned short*)w2_ + (e - 448) * 8;
      glds16d(src, (unsigned)(L_MIX + i * 1024));
    }
    bar();
    asm volatile("s_waitcnt vmcnt(0)" : "+v"(ep) :: "memory");
    const u64 tag = next_tag(ep);
    const unsigned tagq = (unsigned)(tag >> 32);
    if (lane == 0) *(LAS u64*)(L + L_ST + 144) = tag;
    {
      f32x2 pw1[14], pb1[14], pw2[14];
      MIXPREP();
      __builtin_amdgcn_s_setprio(0);
#pragma unroll 1
      for (; tabj < TAB_PER_WG + 1 && (unsigned)__builtin_amdgcn_s_memrealtime() - tstart < (unsigned)(TFIRST - 60); tabj += 2) TABSTEP();
      __builtin_amdgcn_s_setprio(3);
      if (lane == 0) *(LAS unsigned*)(L + L_ST + 152) = tabj;
    }
    for (int sp = 0; sp < 1000 && (unsigned)__builtin_amdgcn_s_memrealtime() - tstart < (unsigned)TFIRST; ++sp) __builtin_amdgcn_s_sleep(2);
    u32x4 v0, v1;
    u64 fl;
    unsigned spins = 0;
    for (;; ++spins) {
      v0 = __builtin_amdgcn_raw_buffer_load_b128(rs, WS_QG + (unsigned)(kvh * 8 + b) * 2048 + lane * 16, 0, AUX_SC1);
      v1 = __builtin_amdgcn_raw_buffer_load_b128(rs, WS_QG + (unsigned)(kvh * 8 + b) * 2048 + 1024 + lane * 16, 0, AUX_SC1);
      fl = __hip_atomic_load((const GAS u64*)(ws + WS_FLAGS) + lane, RLX_AGENT);
      const bool ok = (v0[1] == tagq) & (v0[3] == tagq) & (v1[1] == tagq) & (v1[3] == tagq);
      if (__all(ok) || spins > SPIN_CAP) break;
    }
    *(LAS u32x2*)(L + L_Q + lane * 8) = u32x2{v0[0], v0[2]};
    *(LAS u32x2*)(L + L_Q + 512 + lane * 8) = u32x2{v1[0], v1[2]};
    asm volatile("s_waitcnt lgkmcnt(0)" ::: "memory");
    if (lane == 0) *(volatile LAS unsigned*)(L + L_ST + 136) = 1u;
    STAMPV(12, spins); STAMPV(3, __builtin_amdgcn_s_memrealtime());
    for (unsigned sp = 0; !__all(fl == tag) && sp < SPIN_CAP; ++sp) { __builtin_amdgcn_s_sleep(16); fl = __hip_atomic_load((const GAS u64*)(ws + WS_FLAGS) + lane, RLX_AGENT); }
    if (lane == 0) *(volatile LAS unsigned*)(L + L_ST + 140) = 1u;
    if (lane < min(tabj, TAB_PER_WG)) stw(ws + WS_TAB + (unsigned)(blockIdx.x * TAB_PER_WG + lane) * 4, san(tabv));
  }
  constexpr bool real = true;
  {
  float m = -INFINITY;
  f32x4 acc[9];
#pragma unroll
  for (int e = 0; e < 9; ++e) acc[e] = f32x4{0.f, 0.f, 0.f, 0.f};
  if (real) {
    int tid_ = threadIdx.x; PIN(tid_);
    const int lane = tid_ & 63, c = lane & 15, g = lane >> 4;
    const __amdgpu_buffer_rsrc_t rk = __builtin_amdgcn_make_buffer_rsrc((void*)(kc_ + kvbase), 0, (w == 7 ? ntile : 16 * NSW) * 4096, 0x00020000);
    const __amdgpu_buffer_rsrc_t rv = __builtin_amdgcn_make_buffer_rsrc((void*)(vc_ + kvbase), 0, (w == 7 ? ntile : 16 * NSW) * 4096, 0x00020000);
    constexpr float SC = 0.088388347648318440f, L2E = 1.4426950408889634f;
    const u32x2 ones = {0x3F803F80u, 0x3F803F80u};
#define ISSUE(R, RS, off, n) do { const int o_ = (off) + (n) * tstep; \
    _Pragma("unroll") for (int i_ = 0; i_ < 4; ++i_) R[i_] = __builtin_amdgcn_raw_buffer_load_b128(RS, o_ + i_ * 1024, 0, 2); __builtin_amdgcn_sched_barrier(0); } while (0)
#define PROCESS(K, V, nn, RE) do { \
    float sc_[4]; \
    _Pragma("unroll") for (int i_ = 0; i_ < 4; ++i_) { const f32x4 d_ = mfma(K[i_], qf, f32x4{0.f, 0.f, 0.f, 0.f}); \
      sc_[i_] = (d_[0] + dpp<ROR12>(d_[1])) + (dpp<ROR8>(d_[2]) + dpp<ROR4>(d_[3])); } \
    _Pragma("unroll") for (int i_ = 0; i_ < 4; ++i_) PIN(sc_[i_]); \
    __builtin_amdgcn_sched_barrier(0); \
    if (RE) ISSUE(K, rk, koff, nn); \
    _Pragma("unroll") for (int j_ = 0; j_ < 2; ++j_) {     \
      const unsigned r_ = pk2bf(sc_[2 * j_], sc_[2 * j_ + 1]); const unsigned t_ = pk2bf(bflo(r_) * SC, bfhi(r_) * SC); \
      sc_[2 * j_] = bflo(t_); sc_[2 * j_ + 1] = bfhi(t_); } \
    const float tm_ = xrow16_max(fmaxf(fmaxf(sc_[0], sc_[1]), fmaxf(sc_[2], sc_[3]))) * L2E; \
    const float mn_ = fmaxf(m, tm_); \
    const float al_ = __builtin_amdgcn_exp2f(m - mn_); m = mn_; \
    u32x2 pa_; \
    _Pragma("unroll") for (int j_ = 0; j_ < 2; ++j_) pa_[j_] = pk2bf(__builtin_amdgcn_exp2f(fmaf(sc_[2 * j_], L2E, -mn_)), __builtin_amdgcn_exp2f(fmaf(sc_[2 * j_ + 1], L2E, -mn_))); \
    const float a0_ = __builtin_bit_cast(float, __builtin_amdgcn_readlane(__builtin_bit_cast(int, al_), 0)), a1_ = __builtin_bit_cast(float, __builtin_amdgcn_readlane(__builtin_bit_cast(int, al_), 1)), \
                a2_ = __builtin_bit_cast(float, __builtin_amdgcn_readlane(__builtin_bit_cast(int, al_), 2)), a3_ = __builtin_bit_cast(float, __builtin_amdgcn_readlane(__builtin_bit_cast(int, al_), 3)); \
    _Pragma("unroll") for (int e_ = 0; e_ < 9; ++e_) { acc[e_][0] *= a0_; acc[e_][1] *= a1_; acc[e_][2] *= a2_; acc[e_][3] *= a3_; } \
    acc[8] = mfma16(pa_, ones, acc[8]); \
    u32x2 bf_[8]; \
    _Pragma("unroll") for (int jj_ = 0; jj_ < 2; ++jj_) _Pragma("unroll") for (int d_ = 0; d_ < 4; ++d_) { \
      bf_[2 * d_][jj_] = __builtin_amdgcn_perm(V[2 * jj_ + 1][d_], V[2 * jj_][d_], 0x05040100u); \
      bf_[2 * d_ + 1][jj_] = __builtin_amdgcn_perm(V[2 * jj_ + 1][d_], V[2 * jj_][d_], 0x07060302u); } \
    _Pragma("unroll") for (int e_ = 0; e_ < 8; ++e_) PIN(bf_[e_]); \
    __builtin_amdgcn_sched_barrier(0); \
    if (RE) ISSUE(V, rv, voff, nn); \
    _Pragma("unroll") for (int e_ = 0; e_ < 8; ++e_) acc[e_] = mfma16(pa_, bf_[e_], acc[e_]); \
    __builtin_amdgcn_sched_barrier(0); \
  } while (0)
    if (__builtin_expect(w == 7, 0)) {
      u32x4 K0[4], V0[4], K1[4], V1[4];
      const int tstep = 4096;
      const int koff = (c >> 2) * 256 + g * 64 + (c & 3) * 16, voff = lane * 16;
      const u32x4 qf = *(LAS u32x4*)(L + L_Q + (c & 3) * 256 + (4 * g + (c >> 2)) * 16);
      __builtin_amdgcn_s_setprio(2);
      ISSUE(K0, rk, koff, nstr * NSW); ISSUE(V0, rv, voff, nstr * NSW); ISSUE(K1, rk, koff, nstr * NSW + 1); ISSUE(V1, rv, voff, nstr * NSW + 1);
      for (int t = nstr * NSW; t < ntile; t += 2) { PROCESS(K0, V0, t + 2, true); if (t + 1 < ntile) PROCESS(K1, V1, t + 3, true); }
      STAMP(11);
      __builtin_amdgcn_s_waitcnt(0x0F70);
    } else {
      const int tstep = NSW * 4096;
      const int koff = (c >> 2) * 256 + g * 64 + (c & 3) * 16 + w * 4096, voff = lane * 16 + w * 4096;
      u32x4 K0[4], V0[4], K1[4], V1[4], K2[4], V2[4], K3[4], V3[4];
      __builtin_amdgcn_s_setprio(1);
      ISSUE(K0, rk, koff, 0); STAMP(1);
      asm volatile("s_barrier" ::: "memory");
      ISSUE(V0, rv, voff, 0); ISSUE(K1, rk, koff, 1); ISSUE(V1, rv, voff, 1);
      ISSUE(K2, rk, koff, 2); ISSUE(V2, rv, voff, 2); ISSUE(K3, rk, koff, 3); ISSUE(V3, rv, voff, 3);
      {
        const GAS unsigned char* kg = (const GAS unsigned char*)(kc_ + kvbase) + koff + 16 * tstep;
        const GAS unsigned char* vg = (const GAS unsigned char*)(vc_ + kvbase) + voff + 16 * tstep;
        if (nstr > 16 && !(QPRE && s == 0)) {
#pragma unroll
          for (int i = 0; i < 4; ++i) glds16(kg + i * 1024, (unsigned)(L_KV + w * 8192 + i * 1024));
        }
        if (nstr > 16 && !(QPRE > 1 && s == 0)) {
#pragma unroll
          for (int i = 0; i < 4; ++i) glds16(vg + i * 1024, (unsigned)(L_KV + w * 8192 + 4096 + i * 1024));
        }
        if (nstr > 17) {
#pragma unroll
          for (int i = 0; i < 4; ++i) glds16(kg + tstep + i * 1024, (unsigned)(L_WT + w * 8192 + i * 1024));
#pragma unroll
          for (int i = 0; i < 4; ++i) glds16(vg + tstep + i * 1024, (unsigned)(L_WT + w * 8192 + 4096 + i * 1024));
        }
      }
      for (unsigned spins = 0; ldl(L + L_ST + 136) == 0u && spins < (1u << 24); ++spins) __builtin_amdgcn_s_sleep(1);
      asm volatile("" ::: "memory");
      const u32x4 qf = *(LAS u32x4*)(L + L_Q + (c & 3) * 256 + (4 * g + (c >> 2)) * 16);
      for (int n = 0; n < 16; n += 4) {
        PROCESS(K0, V0, n + 4, true); PROCESS(K1, V1, n + 5, true); PROCESS(K2, V2, n + 6, true); PROCESS(K3, V3, n + 7, true);
        if (n == 0) {
          asm volatile("s_waitcnt vmcnt(32)" ::: "memory");
          for (int t = 0; t < nstr - 16; ++t) {
            u32x4 KL[4], VL[4];
            LAS unsigned char* lp = L + (t ? L_WT : L_KV) + w * 8192 + lane * 16;
#pragma unroll
            for (int i = 0; i < 4; ++i) { KL[i] = *(LAS u32x4*)(lp + i * 1024); VL[i] = *(LAS u32x4*)(lp + 4096 + i * 1024); }
            PROCESS(KL, VL, 0, false);
          }
        }
      }
      __builtin_amdgcn_s_waitcnt(0x0F70);
      STAMP(4);
    }
  }
  int tid_ = threadIdx.x; PIN(tid_);
  const int tid = tid_, lane = tid & 63, c = lane & 15, g = lane >> 4;
  {
    LAS float* rec = (LAS float*)(L + L_KV + w * 8192);
    if (g == 0) {
#pragma unroll
      for (int r = 0; r < 4; ++r) {
        *(LAS f32x4*)(rec + r * 128 + 8 * c) = f32x4{acc[0][r], acc[1][r], acc[2][r], acc[3][r]};
        *(LAS f32x4*)(rec + r * 128 + 8 * c + 4) = f32x4{acc[4][r], acc[5][r], acc[6][r], acc[7][r]};
      }
      if (c < 4) rec[512 + c] = m;
      if (c == 0) *(LAS f32x4*)(rec + 516) = acc[8];
    }
  }
  if (ldl(L + L_ST + 152) < (unsigned)TAB_PER_WG) {
    __builtin_amdgcn_s_setprio(0);
    f32x2 pw1[14], pb1[14], pw2[14];
    MIXPREP();
    unsigned long long mine = 0;
#pragma unroll 1
    for (;;) {
      tabj = __builtin_amdgcn_readfirstlane(lane == 0 ? (int)__hip_atomic_fetch_add((LAS unsigned*)(L + L_ST + 152), 2u, __ATOMIC_RELAXED, __HIP_MEMORY_SCOPE_WORKGROUP) : 0);
      if (tabj >= TAB_PER_WG) break;
      TABSTEP(); mine |= 3ull << tabj;
    }
    for (unsigned sp = 0; ldl(L + L_ST + 140) == 0u && sp < (1u << 22); ++sp) __builtin_amdgcn_s_sleep(4);
    if (lane < TAB_PER_WG && ((mine >> lane) & 1)) stw(ws + WS_TAB + (unsigned)(blockIdx.x * TAB_PER_WG + lane) * 4, san(tabv));
    STAMP(14);
  }
  if (real) bar();
  STAMP(13);
  {
    const int hd = w >> 1;
    const LAS float* rec = (const LAS float*)(L + L_KV);
    const float mw = rec[(lane & 7) * 2048 + 512 + hd], lw = rec[(lane & 7) * 2048 + 516 + hd];
    float av8[NW];
#pragma unroll
    for (int ww = 0; ww < NW; ++ww) av8[ww] = rec[ww * 2048 + tid];
    float oM = fmaxf(mw, dpp<XOR1>(mw)); oM = fmaxf(oM, dpp<XOR2>(oM)); oM = fmaxf(oM, dpp<XOR7>(oM));
    const float wt = __builtin_amdgcn_exp2f(mw - oM);
    float oL = wt * lw; oL += dpp<XOR1>(oL); oL += dpp<XOR2>(oL); oL += dpp<XOR7>(oL);
    float oA = 0.f, oB = 0.f;
#pragma unroll
    for (int ww = 0; ww < NW; ww += 2) {
      oA = fmaf(__uint_as_float(__builtin_amdgcn_readlane(__float_as_uint(wt), ww)), av8[ww], oA);
      oB = fmaf(__uint_as_float(__builtin_amdgcn_readlane(__float_as_uint(wt), ww + 1)), av8[ww + 1], oB);
    }
    GAS unsigned char* rb = ws + WS_REC + (unsigned)(pair * 4 + s) * REC_B;
    stw(rb + tid * 4, san(oA + oB));
    if ((tid & 127) < 2) stw(rb + 2048 + hd * 8 + (tid & 1) * 4, san((tid & 1) ? oL : oM));
  }
  STAMP(5);
  if (s == 1) {
    const int kvh2 = pair >> 3;
    u32x4 ra[4], rb2[4]; u32x2 rh[4];
    const unsigned r0 = WS_REC + (unsigned)((w * 8 + kvh2) * 4) * REC_B + lane * 32;
    const unsigned h0 = WS_REC + (unsigned)((w * 8 + kvh2) * 4) * REC_B + 2048 + g * 8;
#define RLOAD(j) do { ra[j] = __builtin_amdgcn_raw_buffer_load_b128(rs, r0 + (j) * REC_B, 0, AUX_SC1); rb2[j] = __builtin_amdgcn_raw_buffer_load_b128(rs, r0 + (j) * REC_B + 16, 0, AUX_SC1); \
      rh[j] = __builtin_amdgcn_raw_buffer_load_b64(rs, h0 + (j) * REC_B, 0, AUX_SC1); } while (0)
#define RMISS(j) __any(max(max(max(max(ra[j][0], ra[j][1]), max(ra[j][2], ra[j][3])), max(max(rb2[j][0], rb2[j][1]), max(rb2[j][2], rb2[j][3]))), max(rh[j][0], rh[j][1])) == SENT)
    RLOAD(0); RLOAD(1); RLOAD(2); RLOAD(3);
    __builtin_amdgcn_sched_barrier(0);
    u32x4 wf[16];
    {
      const int ta = L_WT + (8 * g + (c >> 2)) * 128 + (w & 3) * 32 + (c & 3) * 8;
#pragma unroll
      for (int ks = 0; ks < 16; ++ks) {
        const u32x2 b0 = trread(L + ta + ks * 4096), b1 = trread(L + ta + ks * 4096 + 512);
        wf[ks] = u32x4{b0[0], b0[1], b1[0], b1[1]};
      }
#pragma unroll
      for (int ks = 0; ks < 16; ++ks) PIN(wf[ks]);
    }
    {
      bool m0 = RMISS(0), m1 = RMISS(1), m2 = RMISS(2), m3 = RMISS(3);
      for (unsigned spins = 0; (m0 | m1 | m2 | m3) && spins < SPIN_CAP; ++spins) {
        asm volatile("" ::: "memory");
        if (m0) RLOAD(0);
        if (m1) RLOAD(1);
        if (m2) RLOAD(2);
        if (m3) RLOAD(3);
        if (m0) m0 = RMISS(0);
        if (m1) m1 = RMISS(1);
        if (m2) m2 = RMISS(2);
        if (m3) m3 = RMISS(3);
      }
    }
    {
      float mj[4], wj[4];
#pragma unroll
      for (int j = 0; j < 4; ++j) mj[j] = __uint_as_float(rh[j][0]);
      const float M = fmaxf(fmaxf(mj[0], mj[1]), fmaxf(mj[2], mj[3]));
#pragma unroll
      for (int j = 0; j < 4; ++j) wj[j] = __builtin_amdgcn_exp2f(mj[j] - M);
      const float Ls = (wj[0] * __uint_as_float(rh[0][1]) + wj[1] * __uint_as_float(rh[1][1])) + (wj[2] * __uint_as_float(rh[2][1]) + wj[3] * __uint_as_float(rh[3][1]));
      const float inv = __builtin_amdgcn_rcpf(Ls);
      const f32x4 oa = ((__builtin_bit_cast(f32x4, ra[0]) * wj[0] + __builtin_bit_cast(f32x4, ra[1]) * wj[1]) + (__builtin_bit_cast(f32x4, ra[2]) * wj[2] + __builtin_bit_cast(f32x4, ra[3]) * wj[3])) * inv;
      const f32x4 ob = ((__builtin_bit_cast(f32x4, rb2[0]) * wj[0] + __builtin_bit_cast(f32x4, rb2[1]) * wj[1]) + (__builtin_bit_cast(f32x4, rb2[2]) * wj[2] + __builtin_bit_cast(f32x4, rb2[3]) * wj[3])) * inv;
      *(LAS u32x4*)(L + L_OA + w * 1040 + lane * 16) = u32x4{pk2bf(oa[0], oa[1]), pk2bf(oa[2], oa[3]), pk2bf(ob[0], ob[1]), pk2bf(ob[2], ob[3])};
    }
    STAMP(6);
    if (real) bar();
    if (w < 4 || !real) {
      f32x4 a0 = {0.f, 0.f, 0.f, 0.f}, a1 = a0, a2 = a0, a3 = a0;
      const LAS unsigned char* op = L + L_OA + (c & 7) * 1040 + (256 * (c >> 3) + 8 * g) * 2;
#pragma unroll
      for (int t = 0; t < 8; t += 2) {
        const u32x4 x0 = *(const LAS u32x4*)(op + t * 64), x1 = *(const LAS u32x4*)(op + t * 64 + 64);
        a0 = mfma(x0, wf[t], a0); a1 = mfma(x0, wf[t + 8], a1);
        a2 = mfma(x1, wf[t + 1], a2); a3 = mfma(x1, wf[t + 9], a3);
      }
      const f32x4 lo = a0 + a2, hi = a1 + a3;
      const float q0 = swap32(g < 2 ? lo[2] : hi[0], lane), q1 = swap32(g < 2 ? lo[3] : hi[1], lane);
      const unsigned e0 = san((g < 2 ? lo[0] : hi[2]) + q0), e1 = san((g < 2 ? lo[1] : hi[3]) + q1);
      GAS unsigned char* ap = ws + WS_ANS + (unsigned)(((4 * (g & 1) + 2 * (g >> 1)) * 8 + kvh2) * 512 + (pair & 7) * 64 + (w & 3) * 16 + c) * 4;
      if (real) { stw(ap, e0); stw(ap + 8 * 512 * 4, e1); } else { asm volatile("" :: "v"(e0), "v"(e1), "v"(ap)); }
      STAMP(7);
    }
  }
  if (owner) {
    unsigned av[8];
#pragma unroll
    for (int k = 0; k < 8; ++k) av[k] = SENT;
    const float xres = bf2f(((const GAS unsigned short*)x_)[b * 512 + tid]), bb = bf2f(((const GAS unsigned short*)b2_)[0]);
    for (unsigned sp = 0; sp < SPIN_CAP; ++sp) {
      u32x4 tb[3];
#pragma unroll
      for (int i = 0; i < 3; ++i) tb[i] = __builtin_amdgcn_raw_buffer_load_b128(rs, WS_TAB + min(i * 512 + tid, 1215) * 16, 0, AUX_SC1);
      unsigned mx = 0;
#pragma unroll
      for (int i = 0; i < 3; ++i) { mx = max(max(mx, tb[i][0]), max(tb[i][1], max(tb[i][2], tb[i][3]))); *(LAS u32x4*)(L + L_WT + min(i * 512 + tid, 1215) * 16) = tb[i]; }
      if (!__any(mx == SENT)) break;
      __builtin_amdgcn_s_sleep(16);
      asm volatile("" ::: "memory");
    }
    unsigned miss = 0xFFu;
    for (unsigned spins = 0; miss && spins < SPIN_CAP; ++spins) {
#pragma unroll
      for (int k = 0; k < 8; ++k) if (miss & (1u << k)) av[k] = ldw(ws + WS_ANS + (unsigned)((b * 8 + k) * 512 + tid) * 4);
#pragma unroll
      for (int k = 0; k < 8; ++k) if (miss & (1u << k)) { if (!__any(av[k] == SENT)) miss &= ~(1u << k); }
      if (miss & (miss - 1)) __builtin_amdgcn_s_sleep(SL3);
    }
    STAMP(15);
    const float a = ((__uint_as_float(av[0]) + __uint_as_float(av[1])) + (__uint_as_float(av[2]) + __uint_as_float(av[3])))
                  + ((__uint_as_float(av[4]) + __uint_as_float(av[5])) + (__uint_as_float(av[6]) + __uint_as_float(av[7])));
    const float hh = rbf(xres + rbf(a));
    {
      const float sh = __uint_as_float(__builtin_amdgcn_readfirstlane(__float_as_uint(hh)));
      const float d = hh - sh, d2 = d * d;
      float tt = ((lane & 1) ? d2 : d) + dpp<XOR1>((lane & 1) ? d : d2);
      tt += dpp<XOR2>(tt); tt += dpp<ROR4>(tt); tt += dpp<ROR8>(tt); tt = xrow16_sum(tt);
      const float s2 = dpp<XOR1>(tt), dm = tt * (1.f / 64);
      if (lane == 0) *(LAS f32x2*)(L + L_ST + w * 8) = f32x2{sh + dm, fmaxf(s2 - tt * dm, 0.f)};
    }
    bar();
    STAMP(8);
    if (b == 0 && tid == 0) __hip_atomic_store((GAS u64*)(ws + WS_EPOCH), *(LAS u64*)(L + L_ST + 144), RLX_AGENT);
    const f32x2 sw = *(LAS f32x2*)(L + L_ST + (lane & 7) * 8);
    float mean = sw[0]; mean += dpp<XOR1>(mean); mean += dpp<XOR2>(mean); mean += dpp<XOR7>(mean); mean *= 0.125f;
    const float dd = sw[0] - mean;
    float var = fmaf(64.f * dd, dd, sw[1]); var += dpp<XOR1>(var); var += dpp<XOR2>(var); var += dpp<XOR7>(var); var *= (1.f / 512);
    const float m1 = rbf(mean), r1 = __builtin_amdgcn_rcpf(rbf(__builtin_amdgcn_sqrtf(rbf(rbf(var) + 1e-5f))));
    const float m2 = rbf((mean - m1) * r1), r2 = __builtin_amdgcn_rcpf(rbf(__builtin_amdgcn_sqrtf(rbf(rbf(var * r1 * r1) + 1e-5f))));
    const float hp = rbf(rbf(rbf(rbf(hh - m1) * r1) - m2) * r2);
    const unsigned hb = __float_as_uint(hp) >> 16, mag = hb & 0x7FFFu, off = mag - (unsigned)TAB_LO;
    const bool intab = off < (unsigned)TAB_N || mag == 0u;
    const unsigned idx = mag == 0u ? 2u * TAB_N : off + ((hb & 0x8000u) ? (unsigned)TAB_N : 0u);
    float yv = *(LAS float*)(L + L_WT + (intab ? idx : 0u) * 4);
    unsigned long long rest = __builtin_amdgcn_ballot_w64(!intab);
    if (__builtin_expect(rest != 0ull, 0)) {
      f32x2 pw1[14], pb1[14], pw2[14];
      MIXPREP();
      while (rest) {
        const int l0 = __builtin_ctzll(rest);
        rest &= rest - 1;
        const float hx = __uint_as_float(__builtin_amdgcn_readlane(__float_as_uint(hp), l0));
        float tv;
        MIX2(tv, hx, hx);
        if (lane == l0) yv = tv;
      }
    }
    ((GAS __bf16*)out_)[b * 512 + tid] = (__bf16)(hh + rbf(rbf(yv) + bb));
  }
  STAMP(9);
  }
}

extern "C" void kernel_launch(void* const* d_in, const int* in_sizes, int n_in, void* d_out, int out_size, void* d_ws, size_t ws_size, hipStream_t stream) {
  k_fused<<<NWG, NTHR, 0, stream>>>((const unsigned short*)d_in[0], (const unsigned short*)d_in[1], (const unsigned short*)d_in[2], (const unsigned short*)d_in[3],
      (const unsigned short*)d_in[4], (const unsigned short*)d_in[6], (const unsigned short*)d_in[7], (const unsigned short*)d_in[8], (const unsigned short*)d_in[9],
      (unsigned short*)d_out, (unsigned char*)d_ws);
}
```

```cpp
#include <hip/hip_runtime.h>
#include <cstdint>

#define GAS __attribute__((address_space(1)))
#define LAS __attribute__((address_space(3)))
#define RLX_AGENT __ATOMIC_RELAXED, __HIP_MEMORY_SCOPE_AGENT
typedef unsigned u32x4 __attribute__((ext_vector_type(4)));
typedef unsigned u32x2 __attribute__((ext_vector_type(2)));
typedef float f32x4 __attribute__((ext_vector_type(4)));
typedef float f32x2 __attribute__((ext_vector_type(2)));
typedef __bf16 bf16x8 __attribute__((ext_vector_type(8)));
typedef __bf16 bf16x2 __attribute__((ext_vector_type(2)));
typedef short s16x4 __attribute__((ext_vector_type(4)));
typedef unsigned long long u64;

constexpr int TFIRST = 550;
constexpr int NT_Q = 123, NT_O = 121;
constexpr int DPAR = 4;

constexpr int NWG = 256, NTHR = 512, NW = 8, NSW = 7;
constexpr int SK = 8192, DH = 128, FF = 1792;
constexpr int NT_B = (512 - NT_Q - NT_O) / 2;
static_assert(NT_Q - DPAR >= 119 && NT_Q - DPAR >= 112 && NT_O - DPAR >= 112 && NT_Q + NT_O + 2 * NT_B == 512, "tile split");
constexpr unsigned WS_EPOCH = 0, WS_FLAGS = 4096, WS_QG = 8192;
constexpr unsigned WS_REC = 0x50000, REC_B = 2176;
constexpr unsigned WS_ANS = WS_REC + 256 * REC_B;
constexpr int TAB_LO = 0x3880, TAB_N = 2360, TAB_PER_WG = 19;
static_assert(2 * TAB_N + 1 <= NWG * TAB_PER_WG, "table");
constexpr unsigned WS_TAB = WS_ANS + 131072;
constexpr unsigned WS_END = WS_TAB + NWG * TAB_PER_WG * 4;
constexpr unsigned CLR_B = (WS_END - WS_REC) / 64;
static_assert(CLR_B == 691 * 16 && CLR_B * 64 == WS_END - WS_REC, "ws");
constexpr unsigned SENT = 0xFFFFFFFFu;
constexpr int AUX_SC1 = 16;
constexpr unsigned SPIN_CAP = 1u << 16;
constexpr int L_WT = 0;
constexpr int L_KV = 65536;
constexpr int L_R7 = L_KV + 7 * 8192;
constexpr int L_MIX = L_R7 + 2560;
constexpr int L_OA = L_MIX + 11264;
constexpr int L_Q = L_OA + 8320;
constexpr int L_ST = L_Q + 1024;
constexpr int L_END = L_ST + 768;
static_assert(L_END <= 163840, "lds");

__device__ __forceinline__ float bf2f(unsigned b) { return __builtin_bit_cast(float, b << 16); }
__device__ __forceinline__ float bflo(unsigned v) { return __builtin_bit_cast(float, v << 16); }
__device__ __forceinline__ float bfhi(unsigned v) { return __builtin_bit_cast(float, v & 0xffff0000u); }
__device__ __forceinline__ unsigned pk2bf(float lo, float hi) { bf16x2 v = {(__bf16)lo, (__bf16)hi}; return __builtin_bit_cast(unsigned, v); }
__device__ __forceinline__ float rbf(float x) { return (float)(__bf16)x; }
template <int CTRL> __device__ __forceinline__ float dpp(float x) {
  return __builtin_bit_cast(float, __builtin_amdgcn_mov_dpp(__builtin_bit_cast(int, x), CTRL, 0xf, 0xf, true));
}
constexpr int XOR1 = 0xB1, XOR2 = 0x4E, XOR7 = 0x141, ROR4 = 0x124, ROR8 = 0x128, ROR12 = 0x12C;
__device__ __forceinline__ float xrow16_max(float x) {
  auto s = __builtin_amdgcn_permlane16_swap(__float_as_uint(x), __float_as_uint(x), false, false);
  x = fmaxf(__uint_as_float(s[0]), __uint_as_float(s[1]));
  auto t = __builtin_amdgcn_permlane32_swap(__float_as_uint(x), __float_as_uint(x), false, false);
  return fmaxf(__uint_as_float(t[0]), __uint_as_float(t[1]));
}
__device__ __forceinline__ float xrow16_sum(float x) {
  auto s = __builtin_amdgcn_permlane16_swap(__float_as_uint(x), __float_as_uint(x), false, false);
  x = __uint_as_float(s[0]) + __uint_as_float(s[1]);
  auto t = __builtin_amdgcn_permlane32_swap(__float_as_uint(x), __float_as_uint(x), false, false);
  return __uint_as_float(t[0]) + __uint_as_float(t[1]);
}
__device__ __forceinline__ float wave_sum(float x) {
  x += dpp<XOR1>(x); x += dpp<XOR2>(x); x += dpp<XOR7>(x); x += dpp<ROR8>(x);
  return xrow16_sum(x);
}
__device__ __forceinline__ float swap32(float x, int lane) {
  auto t = __builtin_amdgcn_permlane32_swap(__float_as_uint(x), __float_as_uint(x), false, false);
  return __uint_as_float(lane < 32 ? t[1] : t[0]);
}
__device__ __forceinline__ f32x4 mfma(u32x4 a, u32x4 b, f32x4 c) {
  return __builtin_amdgcn_mfma_f32_16x16x32_bf16(__builtin_bit_cast(bf16x8, a), __builtin_bit_cast(bf16x8, b), c, 0, 0, 0);
}
__device__ __forceinline__ f32x4 mfma16(u32x2 a, u32x2 b, f32x4 c) {
  return __builtin_amdgcn_mfma_f32_16x16x16bf16_1k(__builtin_bit_cast(s16x4, a), __builtin_bit_cast(s16x4, b), c, 0, 0, 0);
}
__device__ __forceinline__ void bar() { asm volatile("s_waitcnt lgkmcnt(0)\n\ts_barrier" ::: "memory"); }
__device__ __forceinline__ unsigned san(float f) { const unsigned u = __builtin_bit_cast(unsigned, f); return u == SENT ? 0x7FC00000u : u; }
__device__ __forceinline__ void glds16(const GAS void* gsrc, unsigned lds_dst) {
  unsigned keep;
  asm volatile("s_mov_b32 %0, m0\n\ts_mov_b32 m0, %2\n\ts_nop 0\n\tglobal_load_lds_dwordx4 %1, off nt\n\ts_mov_b32 m0, %0" : "=&s"(keep) : "v"(gsrc), "s"(lds_dst) : "memory");
}
__device__ __forceinline__ void glds16d(const GAS void* gsrc, unsigned lds_dst) {
  unsigned keep;
  asm volatile("s_mov_b32 %0, m0\n\ts_mov_b32 m0, %2\n\ts_nop 0\n\tglobal_load_lds_dwordx4 %1, off\n\ts_mov_b32 m0, %0" : "=&s"(keep) : "v"(gsrc), "s"(lds_dst) : "memory");
}
__device__ __forceinline__ u32x2 trread(LAS unsigned char* p) {
  return __builtin_bit_cast(u32x2, __builtin_amdgcn_ds_read_tr16_b64_v4i16((LAS s16x4*)p));
}
__device__ __forceinline__ unsigned ldw(const GAS unsigned char* p) { return __hip_atomic_load((const GAS unsigned*)p, RLX_AGENT); }
__device__ __forceinline__ void stw(GAS unsigned char* p, unsigned v) { __hip_atomic_store((GAS unsigned*)p, v, RLX_AGENT); }
__device__ __forceinline__ unsigned ldl(LAS unsigned char* p) { return __builtin_amdgcn_readfirstlane(*(volatile LAS unsigned*)p); }
__device__ __forceinline__ u64 next_tag(u64 ep) { return ep * 6364136223846793005ull + 1442695040888963407ull; }
__device__ __forceinline__ bool full4(u32x4 v) { return (v[0] != SENT) & (v[1] != SENT) & (v[2] != SENT) & (v[3] != SENT); }
#define PIN(x) asm volatile("" : "+v"(x))

#define MIXPREP() do { \
  _Pragma("unroll") for (int i = 0; i < 7; ++i) { \
    const u32x2 a_ = *(LAS u32x2*)(L + L_MIX + (i * 256 + lane * 4) * 2); \
    const u32x2 b_ = *(LAS u32x2*)(L + L_MIX + 3584 + (i * 256 + lane * 4) * 2); \
    const u32x2 c_ = *(LAS u32x2*)(L + L_MIX + 7168 + (i * 256 + lane * 4) * 2); \
    pw1[2 * i] = f32x2{bflo(a_[0]), bfhi(a_[0])}; pw1[2 * i + 1] = f32x2{bflo(a_[1]), bfhi(a_[1])}; \
    pb1[2 * i] = f32x2{bflo(b_[0]), bfhi(b_[0])}; pb1[2 * i + 1] = f32x2{bflo(b_[1]), bfhi(b_[1])}; \
    pw2[2 * i] = f32x2{bflo(c_[0]), bfhi(c_[0])}; pw2[2 * i + 1] = f32x2{bflo(c_[1]), bfhi(c_[1])}; } \
  _Pragma("unroll") for (int i = 0; i < 14; ++i) { PIN(pw1[i]); PIN(pb1[i]); PIN(pw2[i]); } } while (0)
#define MIX2(T, H0, H1) do { \
    constexpr float K0c = -2.f * 1.4426950408889634f * 0.7978845608028654f, K1c = K0c * 0.044715f; \
    f32x2 ya_ = {0.f, 0.f}, yb_ = {0.f, 0.f}; \
    _Pragma("unroll") for (int i = 0; i < 14; ++i) { \
      _Pragma("unroll") for (int e2 = 0; e2 < 2; ++e2) { \
        const f32x2 u = pw1[i] * (e2 ? (H1) : (H0)) + pb1[i]; \
        const f32x2 z = u * ((u * u) * K1c + K0c); \
        f32x2 e = {__builtin_amdgcn_exp2f(z[0]), __builtin_amdgcn_exp2f(z[1])}; \
        e = e + 1.f; \
        const f32x2 r = {__builtin_amdgcn_rcpf(e[0]), __builtin_amdgcn_rcpf(e[1])};        \
        if (e2) yb_ = (u * pw2[i]) * r + yb_; else ya_ = (u * pw2[i]) * r + ya_; } } \
    const float ta_ = ya_[0] + ya_[1], tb_ = yb_[0] + yb_[1]; \
    float t_ = ((lane & 1) ? tb_ : ta_) + dpp<XOR1>((lane & 1) ? ta_ : tb_); \
    t_ += dpp<XOR2>(t_); t_ += dpp<ROR4>(t_); t_ += dpp<ROR8>(t_); T = xrow16_sum(t_); } while (0)

__global__ void __launch_bounds__(NTHR, 2) k_fused(const unsigned short* __restrict__ x_, const unsigned short* __restrict__ kc_, const unsigned short* __restrict__ vc_,
    const unsigned short* __restrict__ wq_, const unsigned short* __restrict__ wo_, const unsigned short* __restrict__ w1_, const unsigned short* __restrict__ b1_,
    const unsigned short* __restrict__ w2_, const unsigned short* __restrict__ b2_, unsigned short* out_, unsigned char* ws_) {
  __shared__ __attribute__((aligned(16))) unsigned char smem[L_END];
  LAS unsigned char* L = (LAS unsigned char*)smem;
  GAS unsigned char* ws = (GAS unsigned char*)ws_;
  const int tid = threadIdx.x, lane = tid & 63, w = __builtin_amdgcn_readfirstlane(tid >> 6);
  const int c = lane & 15, g = lane >> 4;
  const int xl = blockIdx.x & 7, y = blockIdx.x >> 3;
  const int s = (xl + y) & 3, kvh = 2 * (y & 3) + (xl >> 2), b = y >> 2;
  const int pair = b * 8 + kvh;
  const bool owner = kvh == b && s == 2;
  const __amdgpu_buffer_rsrc_t rs = __builtin_amdgcn_make_buffer_rsrc((void*)ws_, 0, (int)WS_END, 0x00020000);
  const unsigned tstart = (unsigned)__builtin_amdgcn_s_memrealtime();
  const int pe = (y & 1) ? DPAR : -DPAR;
  const int n0 = NT_Q - pe, n1 = NT_O + pe, n2 = NT_B - pe;
  const int ntile = s == 0 ? n0 : s == 1 ? n1 : s == 2 ? n2 : NT_B + pe, tbase = s == 0 ? 0 : s == 1 ? n0 : s == 2 ? n0 + n1 : n0 + n1 + n2;
  const int nstr = 16 + min(s < 2 ? 1 : 2, (ntile - 112) / 7);
  const size_t kvbase = ((size_t)pair * SK + (size_t)tbase * 16) * DH;
  u64 ep = 0;
  if (__builtin_expect(w == 7 || (s == 0 && w < 4), 0)) asm volatile("global_load_dwordx2 %0, %1, off sc1" : "=v"(ep) : "v"(ws + WS_EPOCH) : "memory");
  if (__builtin_expect(w == 7, 0)) {
    if (lane == 0) { *(volatile LAS unsigned*)(L + L_ST + 136) = 0u; *(volatile LAS unsigned*)(L + L_ST + 140) = 0u; }
  }
  if (__builtin_expect(s == 1, 0)) {
    const GAS unsigned short* wp = (const GAS unsigned short*)wo_ + (size_t)((pair >> 3) * 512 + 64 * w + (lane >> 3)) * 512 + (pair & 7) * 64 + (lane & 7) * 8;
#pragma unroll
    for (int i = 0; i < 8; ++i) glds16(wp + (size_t)i * 8 * 512, (unsigned)(L_WT + (w * 8 + i) * 1024));
  }
  if (__builtin_expect(s == 0, 0)) {
    u32x4 xa[2];
    {
      const GAS unsigned short* xp = (const GAS unsigned short*)x_ + (c & 7) * 512 + 64 * w + 8 * g;
      asm volatile("global_load_dwordx4 %0, %2, off\n\tglobal_load_dwordx4 %1, %2, off offset:64" : "=&v"(xa[0]), "=&v"(xa[1]) : "v"(xp) : "memory");
    }
    const GAS unsigned short* wp = (const GAS unsigned short*)wq_ + (size_t)(64 * w + (lane >> 3)) * 4096 + pair * 64 + (lane & 7) * 8;
#pragma unroll
    for (int i = 0; i < 8; ++i) glds16(wp + (size_t)i * 8 * 4096, (unsigned)(L_WT + (w * 8 + i) * 1024));
    asm volatile("s_waitcnt vmcnt(8)" : "+v"(xa[0]), "+v"(xa[1]), "+v"(ep) :: "memory");
    if (w == 7) {
      const u32x4 sv = {SENT, SENT, SENT, SENT};
#pragma unroll
      for (int i = 0; i < 11; ++i) __builtin_amdgcn_raw_buffer_store_b128(sv, rs, WS_REC + (unsigned)pair * CLR_B + min(i * 64 + lane, 690) * 16, 0, AUX_SC1);
    }
    {
      f32x4 ga = {0.f, 0.f, 0.f, 0.f};
#pragma unroll
      for (int t = 0; t < 2; ++t) ga = mfma(c == 8 ? u32x4{0x3F803F80u, 0x3F803F80u, 0x3F803F80u, 0x3F803F80u} : xa[t], xa[t], ga);
      const float dg = (c & 2) ? ((c & 1) ? ga[3] : ga[2]) : ((c & 1) ? ga[1] : ga[0]);
      LAS float* sp = (LAS float*)(L + L_ST + 256) + w * 16;
      if (c < 8 && g == (c >> 2)) sp[2 * c] = dg;
      if (c < 8 && g == 2) sp[2 * c + 1] = ga[0];
    }
    bar();
    {
      f32x2 st = {0.f, 0.f};
#pragma unroll
      for (int ww = 0; ww < 8; ++ww) st += *(const LAS f32x2*)(L + L_ST + 256 + ww * 64 + (c & 7) * 8);
      const float mean = st[1] * (1.f / 512);
      const float mb = rbf(mean), vb = rbf(fmaxf(st[0] * (1.f / 512) - mean * mean, 0.f));
      const float rd = 1.f / rbf(sqrtf(rbf(vb + 1e-5f)));
#pragma unroll
      for (int t = 0; t < 2; ++t)
#pragma unroll
        for (int k = 0; k < 4; ++k) xa[t][k] = pk2bf(rbf(bflo(xa[t][k]) - mb) * rd, rbf(bfhi(xa[t][k]) - mb) * rd);
    }
    if (w < 7) {
      const GAS unsigned char* kg = (const GAS unsigned char*)(kc_ + kvbase) + (c >> 2) * 256 + g * 64 + (c & 3) * 16 + (w + 16 * NSW) * 4096;
#pragma unroll
      for (int i = 0; i < 4; ++i) glds16(kg + i * 1024, (unsigned)(L_KV + w * 8192 + i * 1024));
      asm volatile("s_waitcnt vmcnt(4)" ::: "memory");
    } else {
      asm volatile("s_waitcnt vmcnt(0)" ::: "memory");
    }
    f32x4 qa[4];
#pragma unroll
    for (int j = 0; j < 4; ++j) qa[j] = f32x4{0.f, 0.f, 0.f, 0.f};
#pragma unroll
    for (int t = 0; t < 2; ++t) {
      const int ta = L_WT + (64 * w + 32 * t + 8 * g + (c >> 2)) * 128 + (c & 3) * 8;
#pragma unroll
      for (int j = 0; j < 4; ++j) {
        const u32x2 b0 = trread(L + ta + j * 32), b1 = trread(L + ta + j * 32 + 512);
        qa[j] = mfma(xa[t], u32x4{b0[0], b0[1], b1[0], b1[1]}, qa[j]);
      }
    }
    asm volatile("s_waitcnt lgkmcnt(0)" ::: "memory");
    LAS float* red = (LAS float*)(L + L_WT + w * 8192);
    if (g < 2) {
#pragma unroll
      for (int j = 0; j < 4; ++j)
#pragma unroll
        for (int i = 0; i < 4; ++i) red[(4 * g + i) * 64 + j * 16 + c] = qa[j][i];
    }
    bar();
    if (w < 4) {
      const int bq = 2 * w + (lane >> 5), p = lane & 31;
      f32x2 q2 = {0.f, 0.f};
#pragma unroll
      for (int ww = 0; ww < 8; ++ww) q2 += *(const LAS f32x2*)(L + L_WT + ww * 8192 + (bq * 64 + 2 * p) * 4);
      __hip_atomic_store((GAS u64*)(ws + WS_QG) + ((pair >> 3) * 8 + bq) * 256 + (pair & 7) * 32 + p, (next_tag(ep) & 0xFFFFFFFF00000000ull) | pk2bf(q2[0], q2[1]), RLX_AGENT);
      if (tid == 0) __hip_atomic_store((GAS u64*)(ws + WS_FLAGS) + pair, next_tag(ep), RLX_AGENT);
    }
  }
  int tabj = 0;
  float tabv = 0.f;
#define TABSTEP() do { const int e = blockIdx.x * TAB_PER_WG + tabj; \
        const int f0 = e < TAB_N ? TAB_LO + e : e < 2 * TAB_N ? 0x8000 + TAB_LO + e - TAB_N : 0; \
        const int f1 = e + 1 < TAB_N ? TAB_LO + e + 1 : e + 1 < 2 * TAB_N ? 0x8000 + TAB_LO + e + 1 - TAB_N : 0; \
        float tv; MIX2(tv, bf2f(f0), bf2f(f1)); if ((lane >> 1) == (tabj >> 1)) tabv = tv; } while (0)
  if (__builtin_expect(w == 7, 0)) {
    __builtin_amdgcn_s_setprio(3);
#pragma unroll
    for (int i = 0; i < 11; ++i) {
      const int e = min(i * 64 + lane, 671);
      const GAS unsigned short* src = e < 224 ? (const GAS unsigned short*)w1_ + e * 8 : e < 448 ? (const GAS unsigned short*)b1_ + (e - 224) * 8 : (const GAS unsigned short*)w2_ + (e - 448) * 8;
      glds16d(src, (unsigned)(L_MIX + i * 1024));
    }
    bar();
    asm volatile("s_waitcnt vmcnt(0)" : "+v"(ep) :: "memory");
    const u64 tag = next_tag(ep);
    const unsigned tagq = (unsigned)(tag >> 32);
    if (lane == 0) *(LAS u64*)(L + L_ST + 144) = tag;
    {
      f32x2 pw1[14], pb1[14], pw2[14];
      MIXPREP();
      __builtin_amdgcn_s_setprio(0);
#pragma unroll 1
      for (; tabj < TAB_PER_WG + 1 && (unsigned)__builtin_amdgcn_s_memrealtime() - tstart < (unsigned)(TFIRST - 60); tabj += 2) TABSTEP();
      __builtin_amdgcn_s_setprio(3);
      if (lane == 0) *(LAS unsigned*)(L + L_ST + 152) = tabj;
    }
    for (int sp = 0; sp < 1000 && (unsigned)__builtin_amdgcn_s_memrealtime() - tstart < (unsigned)TFIRST; ++sp) __builtin_amdgcn_s_sleep(2);
    u64 qv[4], fl;
    for (unsigned spins = 0;; ++spins) {
#pragma unroll
      for (int i = 0; i < 4; ++i) qv[i] = __hip_atomic_load((const GAS u64*)(ws + WS_QG) + (kvh * 8 + b) * 256 + i * 64 + lane, RLX_AGENT);
      fl = __hip_atomic_load((const GAS u64*)(ws + WS_FLAGS) + lane, RLX_AGENT);
      const bool ok = ((unsigned)(qv[0] >> 32) == tagq) & ((unsigned)(qv[1] >> 32) == tagq) & ((unsigned)(qv[2] >> 32) == tagq) & ((unsigned)(qv[3] >> 32) == tagq);
      if (__all(ok) || spins > SPIN_CAP) break;
    }
#pragma unroll
    for (int i = 0; i < 4; ++i) *(LAS unsigned*)(L + L_Q + (i * 64 + lane) * 4) = (unsigned)qv[i];
    asm volatile("s_waitcnt lgkmcnt(0)" ::: "memory");
    if (lane == 0) *(volatile LAS unsigned*)(L + L_ST + 136) = 1u;
    for (unsigned sp = 0; !__all(fl == tag) && sp < SPIN_CAP; ++sp) { __builtin_amdgcn_s_sleep(16); fl = __hip_atomic_load((const GAS u64*)(ws + WS_FLAGS) + lane, RLX_AGENT); }
    if (lane == 0) *(volatile LAS unsigned*)(L + L_ST + 140) = 1u;
    if (lane < min(tabj, TAB_PER_WG)) stw(ws + WS_TAB + (unsigned)(blockIdx.x * TAB_PER_WG + lane) * 4, san(tabv));
  }
  {
  float m = -INFINITY;
  f32x4 acc[9];
#pragma unroll
  for (int e = 0; e < 9; ++e) acc[e] = f32x4{0.f, 0.f, 0.f, 0.f};
  {
    int tid_ = threadIdx.x; PIN(tid_);
    const int lane = tid_ & 63, c = lane & 15, g = lane >> 4;
    const __amdgpu_buffer_rsrc_t rk = __builtin_amdgcn_make_buffer_rsrc((void*)(kc_ + kvbase), 0, (w == 7 ? ntile : 16 * NSW) * 4096, 0x00020000);
    const __amdgpu_buffer_rsrc_t rv = __builtin_amdgcn_make_buffer_rsrc((void*)(vc_ + kvbase), 0, (w == 7 ? ntile : 16 * NSW) * 4096, 0x00020000);
    constexpr float SC = 0.088388347648318440f, L2E = 1.4426950408889634f;
    const u32x2 ones = {0x3F803F80u, 0x3F803F80u};
#define ISSUE(R, RS, off, n) do { const int o_ = (off) + (n) * tstep; \
    _Pragma("unroll") for (int i_ = 0; i_ < 4; ++i_) R[i_] = __builtin_amdgcn_raw_buffer_load_b128(RS, o_ + i_ * 1024, 0, 2); __builtin_amdgcn_sched_barrier(0); } while (0)
#define PROCESS(K, V, nn, RE) do { \
    float sc_[4]; \
    _Pragma("unroll") for (int i_ = 0; i_ < 4; ++i_) { const f32x4 d_ = mfma(K[i_], qf, f32x4{0.f, 0.f, 0.f, 0.f}); \
      sc_[i_] = (d_[0] + dpp<ROR12>(d_[1])) + (dpp<ROR8>(d_[2]) + dpp<ROR4>(d_[3])); } \
    _Pragma("unroll") for (int i_ = 0; i_ < 4; ++i_) PIN(sc_[i_]); \
    __builtin_amdgcn_sched_barrier(0); \
    if (RE) ISSUE(K, rk, koff, nn); \
    _Pragma("unroll") for (int j_ = 0; j_ < 2; ++j_) {     \
      const unsigned r_ = pk2bf(sc_[2 * j_], sc_[2 * j_ + 1]); const unsigned t_ = pk2bf(bflo(r_) * SC, bfhi(r_) * SC); \
      sc_[2 * j_] = bflo(t_); sc_[2 * j_ + 1] = bfhi(t_); } \
    const float tm_ = xrow16_max(fmaxf(fmaxf(sc_[0], sc_[1]), fmaxf(sc_[2], sc_[3]))) * L2E; \
    const float mn_ = fmaxf(m, tm_); \
    const float al_ = __builtin_amdgcn_exp2f(m - mn_); m = mn_; \
    u32x2 pa_; \
    _Pragma("unroll") for (int j_ = 0; j_ < 2; ++j_) pa_[j_] = pk2bf(__builtin_amdgcn_exp2f(fmaf(sc_[2 * j_], L2E, -mn_)), __builtin_amdgcn_exp2f(fmaf(sc_[2 * j_ + 1], L2E, -mn_))); \
    const float a0_ = __builtin_bit_cast(float, __builtin_amdgcn_readlane(__builtin_bit_cast(int, al_), 0)), a1_ = __builtin_bit_cast(float, __builtin_amdgcn_readlane(__builtin_bit_cast(int, al_), 1)), \
                a2_ = __builtin_bit_cast(float, __builtin_amdgcn_readlane(__builtin_bit_cast(int, al_), 2)), a3_ = __builtin_bit_cast(float, __builtin_amdgcn_readlane(__builtin_bit_cast(int, al_), 3)); \
    _Pragma("unroll") for (int e_ = 0; e_ < 9; ++e_) { acc[e_][0] *= a0_; acc[e_][1] *= a1_; acc[e_][2] *= a2_; acc[e_][3] *= a3_; } \
    acc[8] = mfma16(pa_, ones, acc[8]); \
    u32x2 bf_[8]; \
    _Pragma("unroll") for (int jj_ = 0; jj_ < 2; ++jj_) _Pragma("unroll") for (int d_ = 0; d_ < 4; ++d_) { \
      bf_[2 * d_][jj_] = __builtin_amdgcn_perm(V[2 * jj_ + 1][d_], V[2 * jj_][d_], 0x05040100u); \
      bf_[2 * d_ + 1][jj_] = __builtin_amdgcn_perm(V[2 * jj_ + 1][d_], V[2 * jj_][d_], 0x07060302u); } \
    _Pragma("unroll") for (int e_ = 0; e_ < 8; ++e_) PIN(bf_[e_]); \
    __builtin_amdgcn_sched_barrier(0); \
    if (RE) ISSUE(V, rv, voff, nn); \
    _Pragma("unroll") for (int e_ = 0; e_ < 8; ++e_) acc[e_] = mfma16(pa_, bf_[e_], acc[e_]); \
    __builtin_amdgcn_sched_barrier(0); \
  } while (0)
    if (__builtin_expect(w == 7, 0)) {
      u32x4 K0[4], V0[4], K1[4], V1[4];
      const int tstep = 4096;
      const int koff = (c >> 2) * 256 + g * 64 + (c & 3) * 16, voff = lane * 16;
      const u32x4 qf = *(LAS u32x4*)(L + L_Q + (c & 3) * 256 + (4 * g + (c >> 2)) * 16);
      __builtin_amdgcn_s_setprio(2);
      ISSUE(K0, rk, koff, nstr * NSW); ISSUE(V0, rv, voff, nstr * NSW); ISSUE(K1, rk, koff, nstr * NSW + 1); ISSUE(V1, rv, voff, nstr * NSW + 1);
      for (int t = nstr * NSW; t < ntile; t += 2) { PROCESS(K0, V0, t + 2, true); if (t + 1 < ntile) PROCESS(K1, V1, t + 3, true); }
      __builtin_amdgcn_s_waitcnt(0x0F70);
    } else {
      const int tstep = NSW * 4096;
      const int koff = (c >> 2) * 256 + g * 64 + (c & 3) * 16 + w * 4096, voff = lane * 16 + w * 4096;
      u32x4 K0[4], V0[4], K1[4], V1[4], K2[4], V2[4], K3[4], V3[4];
      __builtin_amdgcn_s_setprio(1);
      ISSUE(K0, rk, koff, 0);      asm volatile("s_barrier" ::: "memory");
      ISSUE(V0, rv, voff, 0); ISSUE(K1, rk, koff, 1); ISSUE(V1, rv, voff, 1);
      ISSUE(K2, rk, koff, 2); ISSUE(V2, rv, voff, 2); ISSUE(K3, rk, koff, 3); ISSUE(V3, rv, voff, 3);
      {
        const GAS unsigned char* kg = (const GAS unsigned char*)(kc_ + kvbase) + koff + 16 * tstep;
        const GAS unsigned char* vg = (const GAS unsigned char*)(vc_ + kvbase) + voff + 16 * tstep;
        if (nstr > 16 && s != 0) {
#pragma unroll
          for (int i = 0; i < 4; ++i) glds16(kg + i * 1024, (unsigned)(L_KV + w * 8192 + i * 1024));
        }
        if (nstr > 16) {
#pragma unroll
          for (int i = 0; i < 4; ++i) glds16(vg + i * 1024, (unsigned)(L_KV + w * 8192 + 4096 + i * 1024));
        }
        if (nstr > 17) {
#pragma unroll
          for (int i = 0; i < 4; ++i) glds16(kg + tstep + i * 1024, (unsigned)(L_WT + w * 8192 + i * 1024));
#pragma unroll
          for (int i = 0; i < 4; ++i) glds16(vg + tstep + i * 1024, (unsigned)(L_WT + w * 8192 + 4096 + i * 1024));
        }
      }
      for (unsigned spins = 0; ldl(L + L_ST + 136) == 0u && spins < (1u << 24); ++spins) __builtin_amdgcn_s_sleep(1);
      asm volatile("" ::: "memory");
      const u32x4 qf = *(LAS u32x4*)(L + L_Q + (c & 3) * 256 + (4 * g + (c >> 2)) * 16);
      for (int n = 0; n < 16; n += 4) {
        PROCESS(K0, V0, n + 4, true); PROCESS(K1, V1, n + 5, true); PROCESS(K2, V2, n + 6, true); PROCESS(K3, V3, n + 7, true);
        if (n == 0) {
          asm volatile("s_waitcnt vmcnt(32)" ::: "memory");
          for (int t = 0; t < nstr - 16; ++t) {
            u32x4 KL[4], VL[4];
            LAS unsigned char* lp = L + (t ? L_WT : L_KV) + w * 8192 + lane * 16;
#pragma unroll
            for (int i = 0; i < 4; ++i) { KL[i] = *(LAS u32x4*)(lp + i * 1024); VL[i] = *(LAS u32x4*)(lp + 4096 + i * 1024); }
            PROCESS(KL, VL, 0, false);
          }
        }
      }
      __builtin_amdgcn_s_waitcnt(0x0F70);
    }
  }
  int tid_ = threadIdx.x; PIN(tid_);
  const int tid = tid_, lane = tid & 63, c = lane & 15, g = lane >> 4;
  {
    LAS float* rec = (LAS float*)(L + L_KV + w * 8192);
    if (g == 0) {
#pragma unroll
      for (int r = 0; r < 4; ++r) {
        *(LAS f32x4*)(rec + r * 128 + 8 * c) = f32x4{acc[0][r], acc[1][r], acc[2][r], acc[3][r]};
        *(LAS f32x4*)(rec + r * 128 + 8 * c + 4) = f32x4{acc[4][r], acc[5][r], acc[6][r], acc[7][r]};
      }
      if (c < 4) rec[512 + c] = m;
      if (c == 0) *(LAS f32x4*)(rec + 516) = acc[8];
    }
  }
  if (ldl(L + L_ST + 152) < (unsigned)TAB_PER_WG) {
    __builtin_amdgcn_s_setprio(0);
    f32x2 pw1[14], pb1[14], pw2[14];
    MIXPREP();
    unsigned long long mine = 0;
#pragma unroll 1
    for (;;) {
      tabj = __builtin_amdgcn_readfirstlane(lane == 0 ? (int)__hip_atomic_fetch_add((LAS unsigned*)(L + L_ST + 152), 2u, __ATOMIC_RELAXED, __HIP_MEMORY_SCOPE_WORKGROUP) : 0);
      if (tabj >= TAB_PER_WG) break;
      TABSTEP(); mine |= 3ull << tabj;
    }
    for (unsigned sp = 0; ldl(L + L_ST + 140) == 0u && sp < (1u << 22); ++sp) __builtin_amdgcn_s_sleep(4);
    if (lane < TAB_PER_WG && ((mine >> lane) & 1)) stw(ws + WS_TAB + (unsigned)(blockIdx.x * TAB_PER_WG + lane) * 4, san(tabv));
  }
  bar();
  {
    const int hd = w >> 1;
    const LAS float* rec = (const LAS float*)(L + L_KV);
    const float mw = rec[(lane & 7) * 2048 + 512 + hd], lw = rec[(lane & 7) * 2048 + 516 + hd];
    float av8[NW];
#pragma unroll
    for (int ww = 0; ww < NW; ++ww) av8[ww] = rec[ww * 2048 + tid];
    float oM = fmaxf(mw, dpp<XOR1>(mw)); oM = fmaxf(oM, dpp<XOR2>(oM)); oM = fmaxf(oM, dpp<XOR7>(oM));
    const float wt = __builtin_amdgcn_exp2f(mw - oM);
    float oL = wt * lw; oL += dpp<XOR1>(oL); oL += dpp<XOR2>(oL); oL += dpp<XOR7>(oL);
    float oA = 0.f, oB = 0.f;
#pragma unroll
    for (int ww = 0; ww < NW; ww += 2) {
      oA = fmaf(__uint_as_float(__builtin_amdgcn_readlane(__float_as_uint(wt), ww)), av8[ww], oA);
      oB = fmaf(__uint_as_float(__builtin_amdgcn_readlane(__float_as_uint(wt), ww + 1)), av8[ww + 1], oB);
    }
    GAS unsigned char* rb = ws + WS_REC + (unsigned)(pair * 4 + s) * REC_B;
    stw(rb + tid * 4, san(oA + oB));
    if ((tid & 127) < 2) stw(rb + 2048 + hd * 8 + (tid & 1) * 4, san((tid & 1) ? oL : oM));
  }
  if (s == 1) {
    const int kvh2 = pair >> 3;
    u32x4 ra[4], rb2[4]; u32x2 rh[4];
    const unsigned r0 = WS_REC + (unsigned)((w * 8 + kvh2) * 4) * REC_B + lane * 32;
    const unsigned h0 = WS_REC + (unsigned)((w * 8 + kvh2) * 4) * REC_B + 2048 + g * 8;
#define RLOAD(j) do { ra[j] = __builtin_amdgcn_raw_buffer_load_b128(rs, r0 + (j) * REC_B, 0, AUX_SC1); rb2[j] = __builtin_amdgcn_raw_buffer_load_b128(rs, r0 + (j) * REC_B + 16, 0, AUX_SC1); \
      rh[j] = __builtin_amdgcn_raw_buffer_load_b64(rs, h0 + (j) * REC_B, 0, AUX_SC1); } while (0)
#define RMISS(j) __any(max(max(max(max(ra[j][0], ra[j][1]), max(ra[j][2], ra[j][3])), max(max(rb2[j][0], rb2[j][1]), max(rb2[j][2], rb2[j][3]))), max(rh[j][0], rh[j][1])) == SENT)
    RLOAD(0); RLOAD(1); RLOAD(2); RLOAD(3);
    __builtin_amdgcn_sched_barrier(0);
    u32x4 wf[16];
    {
      const int ta = L_WT + (8 * g + (c >> 2)) * 128 + (w & 3) * 32 + (c & 3) * 8;
#pragma unroll
      for (int ks = 0; ks < 16; ++ks) {
        const u32x2 b0 = trread(L + ta + ks * 4096), b1 = trread(L + ta + ks * 4096 + 512);
        wf[ks] = u32x4{b0[0], b0[1], b1[0], b1[1]};
      }
#pragma unroll
      for (int ks = 0; ks < 16; ++ks) PIN(wf[ks]);
    }
    {
      bool m0 = RMISS(0), m1 = RMISS(1), m2 = RMISS(2), m3 = RMISS(3);
      for (unsigned spins = 0; (m0 | m1 | m2 | m3) && spins < SPIN_CAP; ++spins) {
        asm volatile("" ::: "memory");
        if (m0) RLOAD(0);
        if (m1) RLOAD(1);
        if (m2) RLOAD(2);
        if (m3) RLOAD(3);
        if (m0) m0 = RMISS(0);
        if (m1) m1 = RMISS(1);
        if (m2) m2 = RMISS(2);
        if (m3) m3 = RMISS(3);
      }
    }
    {
      float mj[4], wj[4];
#pragma unroll
      for (int j = 0; j < 4; ++j) mj[j] = __uint_as_float(rh[j][0]);
      const float M = fmaxf(fmaxf(mj[0], mj[1]), fmaxf(mj[2], mj[3]));
#pragma unroll
      for (int j = 0; j < 4; ++j) wj[j] = __builtin_amdgcn_exp2f(mj[j] - M);
      const float Ls = (wj[0] * __uint_as_float(rh[0][1]) + wj[1] * __uint_as_float(rh[1][1])) + (wj[2] * __uint_as_float(rh[2][1]) + wj[3] * __uint_as_float(rh[3][1]));
      const float inv = __builtin_amdgcn_rcpf(Ls);
      const f32x4 oa = ((__builtin_bit_cast(f32x4, ra[0]) * wj[0] + __builtin_bit_cast(f32x4, ra[1]) * wj[1]) + (__builtin_bit_cast(f32x4, ra[2]) * wj[2] + __builtin_bit_cast(f32x4, ra[3]) * wj[3])) * inv;
      const f32x4 ob = ((__builtin_bit_cast(f32x4, rb2[0]) * wj[0] + __builtin_bit_cast(f32x4, rb2[1]) * wj[1]) + (__builtin_bit_cast(f32x4, rb2[2]) * wj[2] + __builtin_bit_cast(f32x4, rb2[3]) * wj[3])) * inv;
      *(LAS u32x4*)(L + L_OA + w * 1040 + lane * 16) = u32x4{pk2bf(oa[0], oa[1]), pk2bf(oa[2], oa[3]), pk2bf(ob[0], ob[1]), pk2bf(ob[2], ob[3])};
    }
    bar();
    if (w < 4) {
      f32x4 a0 = {0.f, 0.f, 0.f, 0.f}, a1 = a0, a2 = a0, a3 = a0;
      const LAS unsigned char* op = L + L_OA + (c & 7) * 1040 + (256 * (c >> 3) + 8 * g) * 2;
#pragma unroll
      for (int t = 0; t < 8; t += 2) {
        const u32x4 x0 = *(const LAS u32x4*)(op + t * 64), x1 = *(const LAS u32x4*)(op + t * 64 + 64);
        a0 = mfma(x0, wf[t], a0); a1 = mfma(x0, wf[t + 8], a1);
        a2 = mfma(x1, wf[t + 1], a2); a3 = mfma(x1, wf[t + 9], a3);
      }
      const f32x4 lo = a0 + a2, hi = a1 + a3;
      const float q0 = swap32(g < 2 ? lo[2] : hi[0], lane), q1 = swap32(g < 2 ? lo[3] : hi[1], lane);
      const unsigned e0 = san((g < 2 ? lo[0] : hi[2]) + q0), e1 = san((g < 2 ? lo[1] : hi[3]) + q1);
      GAS unsigned char* ap = ws + WS_ANS + (unsigned)(((4 * (g & 1) + 2 * (g >> 1)) * 8 + kvh2) * 512 + (pair & 7) * 64 + (w & 3) * 16 + c) * 4;
      stw(ap, e0); stw(ap + 8 * 512 * 4, e1);
    }
  }
  if (owner) {
    unsigned av[8];
#pragma unroll
    for (int k = 0; k < 8; ++k) av[k] = SENT;
    const float xres = bf2f(((const GAS unsigned short*)x_)[b * 512 + tid]), bb = bf2f(((const GAS unsigned short*)b2_)[0]);
    for (unsigned sp = 0; sp < SPIN_CAP; ++sp) {
      u32x4 tb[3];
#pragma unroll
      for (int i = 0; i < 3; ++i) tb[i] = __builtin_amdgcn_raw_buffer_load_b128(rs, WS_TAB + min(i * 512 + tid, 1215) * 16, 0, AUX_SC1);
      unsigned mx = 0;
#pragma unroll
      for (int i = 0; i < 3; ++i) { mx = max(max(mx, tb[i][0]), max(tb[i][1], max(tb[i][2], tb[i][3]))); *(LAS u32x4*)(L + L_WT + min(i * 512 + tid, 1215) * 16) = tb[i]; }
      if (!__any(mx == SENT)) break;
      __builtin_amdgcn_s_sleep(16);
      asm volatile("" ::: "memory");
    }
    unsigned miss = 0xFFu;
    for (unsigned spins = 0; miss && spins < SPIN_CAP; ++spins) {
#pragma unroll
      for (int k = 0; k < 8; ++k) if (miss & (1u << k)) av[k] = ldw(ws + WS_ANS + (unsigned)((b * 8 + k) * 512 + tid) * 4);
#pragma unroll
      for (int k = 0; k < 8; ++k) if (miss & (1u << k)) { if (!__any(av[k] == SENT)) miss &= ~(1u << k); }
      if (miss & (miss - 1)) __builtin_amdgcn_s_sleep(4);
    }
    const float a = ((__uint_as_float(av[0]) + __uint_as_float(av[1])) + (__uint_as_float(av[2]) + __uint_as_float(av[3])))
                  + ((__uint_as_float(av[4]) + __uint_as_float(av[5])) + (__uint_as_float(av[6]) + __uint_as_float(av[7])));
    const float hh = rbf(xres + rbf(a));
    {
      const float sh = __uint_as_float(__builtin_amdgcn_readfirstlane(__float_as_uint(hh)));
      const float d = hh - sh, d2 = d * d;
      float tt = ((lane & 1) ? d2 : d) + dpp<XOR1>((lane & 1) ? d : d2);
      tt += dpp<XOR2>(tt); tt += dpp<ROR4>(tt); tt += dpp<ROR8>(tt); tt = xrow16_sum(tt);
      const float s2 = dpp<XOR1>(tt), dm = tt * (1.f / 64);
      if (lane == 0) *(LAS f32x2*)(L + L_ST + w * 8) = f32x2{sh + dm, fmaxf(s2 - tt * dm, 0.f)};
    }
    bar();
    if (b == 0 && tid == 0) __hip_atomic_store((GAS u64*)(ws + WS_EPOCH), *(LAS u64*)(L + L_ST + 144), RLX_AGENT);
    const f32x2 sw = *(LAS f32x2*)(L + L_ST + (lane & 7) * 8);
    float mean = sw[0]; mean += dpp<XOR1>(mean); mean += dpp<XOR2>(mean); mean += dpp<XOR7>(mean); mean *= 0.125f;
    const float dd = sw[0] - mean;
    float var = fmaf(64.f * dd, dd, sw[1]); var += dpp<XOR1>(var); var += dpp<XOR2>(var); var += dpp<XOR7>(var); var *= (1.f / 512);
    const float m1 = rbf(mean), r1 = __builtin_amdgcn_rcpf(rbf(__builtin_amdgcn_sqrtf(rbf(rbf(var) + 1e-5f))));
    const float m2 = rbf((mean - m1) * r1), r2 = __builtin_amdgcn_rcpf(rbf(__builtin_amdgcn_sqrtf(rbf(rbf(var * r1 * r1) + 1e-5f))));
    const float hp = rbf(rbf(rbf(rbf(hh - m1) * r1) - m2) * r2);
    const unsigned hb = __float_as_uint(hp) >> 16, mag = hb & 0x7FFFu, off = mag - (unsigned)TAB_LO;
    const bool intab = off < (unsigned)TAB_N || mag == 0u;
    const unsigned idx = mag == 0u ? 2u * TAB_N : off + ((hb & 0x8000u) ? (unsigned)TAB_N : 0u);
    float yv = *(LAS float*)(L + L_WT + (intab ? idx : 0u) * 4);
    unsigned long long rest = __builtin_amdgcn_ballot_w64(!intab);
    if (__builtin_expect(rest != 0ull, 0)) {
      f32x2 pw1[14], pb1[14], pw2[14];
      MIXPREP();
      while (rest) {
        const int l0 = __builtin_ctzll(rest);
        rest &= rest - 1;
        const float hx = __uint_as_float(__builtin_amdgcn_readlane(__float_as_uint(hp), l0));
        float tv;
        MIX2(tv, hx, hx);
        if (lane == l0) yv = tv;
      }
    }
    ((GAS __bf16*)out_)[b * 512 + tid] = (__bf16)(hh + rbf(rbf(yv) + bb));
  }
  }
}

extern "C" void kernel_launch(void* const* d_in, const int* in_sizes, int n_in, void* d_out, int out_size, void* d_ws, size_t ws_size, hipStream_t stream) {
  k_fused<<<NWG, NTHR, 0, stream>>>((const unsigned short*)d_in[0], (const unsigned short*)d_in[1], (const unsigned short*)d_in[2], (const unsigned short*)d_in[3],
      (const unsigned short*)d_in[4], (const unsigned short*)d_in[6], (const unsigned short*)d_in[7], (const unsigned short*)d_in[8], (const unsigned short*)d_in[9],
      (unsigned short*)d_out, (unsigned char*)d_ws);
}
```

```cpp
#include <hip/hip_runtime.h>
#include <cstdint>

#define GAS __attribute__((address_space(1)))
#define LAS __attribute__((address_space(3)))
#define RLX_AGENT __ATOMIC_RELAXED, __HIP_MEMORY_SCOPE_AGENT
typedef unsigned u32x4 __attribute__((ext_vector_type(4)));
typedef unsigned u32x2 __attribute__((ext_vector_type(2)));
typedef float f32x4 __attribute__((ext_vector_type(4)));
typedef float f32x2 __attribute__((ext_vector_type(2)));
typedef __bf16 bf16x8 __attribute__((ext_vector_type(8)));
typedef __bf16 bf16x2 __attribute__((ext_vector_type(2)));
typedef short s16x4 __attribute__((ext_vector_type(4)));
typedef unsigned long long u64;

constexpr int TFIRST = 550;
constexpr int NT_Q = 123, NT_O = 121;
constexpr int DPAR = 4;

constexpr int NWG = 256, NTHR = 512, NW = 8, NSW = 7;
constexpr int SK = 8192, DH = 128, FF = 1792;
constexpr int NT_B = (512 - NT_Q - NT_O) / 2;
static_assert(NT_Q - DPAR >= 119 && NT_Q - DPAR >= 112 && NT_O - DPAR >= 112 && NT_Q + NT_O + 2 * NT_B == 512, "tile split");
constexpr unsigned WS_EPOCH = 0, WS_FLAGS = 4096, WS_QG = 8192;
constexpr unsigned WS_REC = 0x50000, REC_B = 2176;
constexpr unsigned WS_ANS = WS_REC + 256 * REC_B;
constexpr int TAB_LO = 0x3780, TAB_N = 2431, TAB_PER_WG = 19;
static_assert(2 * TAB_N + 1 <= NWG * TAB_PER_WG, "table");
constexpr unsigned WS_TAB = WS_ANS + 131072;
constexpr unsigned WS_END = WS_TAB + NWG * TAB_PER_WG * 4;
constexpr unsigned CLR_B = (WS_END - WS_REC) / 64;
static_assert(CLR_B == 691 * 16 && CLR_B * 64 == WS_END - WS_REC, "ws");
constexpr unsigned SENT = 0xFFFFFFFFu;
constexpr int AUX_SC1 = 16;
constexpr unsigned SPIN_CAP = 1u << 16;
constexpr int L_WT = 0;
constexpr int L_KV = 65536;
constexpr int L_R7 = L_KV + 7 * 8192;
constexpr int L_MIX = L_R7 + 2560;
constexpr int L_OA = L_MIX + 11264;
constexpr int L_Q = L_OA + 8320;
constexpr int L_ST = L_Q + 1024;
constexpr int L_END = L_ST + 768;
static_assert(L_END <= 163840, "lds");

__device__ __forceinline__ float bf2f(unsigned b) { return __builtin_bit_cast(float, b << 16); }
__device__ __forceinline__ float bflo(unsigned v) { return __builtin_bit_cast(float, v << 16); }
__device__ __forceinline__ float bfhi(unsigned v) { return __builtin_bit_cast(float, v & 0xffff0000u); }
__device__ __forceinline__ unsigned pk2bf(float lo, float hi) { bf16x2 v = {(__bf16)lo, (__bf16)hi}; return __builtin_bit_cast(unsigned, v); }
__device__ __forceinline__ float rbf(float x) { return (float)(__bf16)x; }
template <int CTRL> __device__ __forceinline__ float dpp(float x) {
  return __builtin_bit_cast(float, __builtin_amdgcn_mov_dpp(__builtin_bit_cast(int, x), CTRL, 0xf, 0xf, true));
}
constexpr int XOR1 = 0xB1, XOR2 = 0x4E, XOR7 = 0x141, ROR4 = 0x124, ROR8 = 0x128, ROR12 = 0x12C;
__device__ __forceinline__ float xrow16_max(float x) {
  auto s = __builtin_amdgcn_permlane16_swap(__float_as_uint(x), __float_as_uint(x), false, false);
  x = fmaxf(__uint_as_float(s[0]), __uint_as_float(s[1]));
  auto t = __builtin_amdgcn_permlane32_swap(__float_as_uint(x), __float_as_uint(x), false, false);
  return fmaxf(__uint_as_float(t[0]), __uint_as_float(t[1]));
}
__device__ __forceinline__ float xrow16_sum(float x) {
  auto s = __builtin_amdgcn_permlane16_swap(__float_as_uint(x), __float_as_uint(x), false, false);
  x = __uint_as_float(s[0]) + __uint_as_float(s[1]);
  auto t = __builtin_amdgcn_permlane32_swap(__float_as_uint(x), __float_as_uint(x), false, false);
  return __uint_as_float(t[0]) + __uint_as_float(t[1]);
}
__device__ __forceinline__ float wave_sum(float x) {
  x += dpp<XOR1>(x); x += dpp<XOR2>(x); x += dpp<XOR7>(x); x += dpp<ROR8>(x);
  return xrow16_sum(x);
}
__device__ __forceinline__ float swap32(float x, int lane) {
  auto t = __builtin_amdgcn_permlane32_swap(__float_as_uint(x), __float_as_uint(x), false, false);
  return __uint_as_float(lane < 32 ? t[1] : t[0]);
}
__device__ __forceinline__ f32x4 mfma(u32x4 a, u32x4 b, f32x4 c) {
  return __builtin_amdgcn_mfma_f32_16x16x32_bf16(__builtin_bit_cast(bf16x8, a), __builtin_bit_cast(bf16x8, b), c, 0, 0, 0);
}
__device__ __forceinline__ f32x4 mfma16(u32x2 a, u32x2 b, f32x4 c) {
  return __builtin_amdgcn_mfma_f32_16x16x16bf16_1k(__builtin_bit_cast(s16x4, a), __builtin_bit_cast(s16x4, b), c, 0, 0, 0);
}
__device__ __forceinline__ void bar() { asm volatile("s_waitcnt lgkmcnt(0)\n\ts_barrier" ::: "memory"); }
__device__ __forceinline__ unsigned san(float f) { const unsigned u = __builtin_bit_cast(unsigned, f); return u == SENT ? 0x7FC00000u : u; }
__device__ __forceinline__ void glds16(const GAS void* gsrc, unsigned lds_dst) {
  unsigned keep;
  asm volatile("s_mov_b32 %0, m0\n\ts_mov_b32 m0, %2\n\ts_nop 0\n\tglobal_load_lds_dwordx4 %1, off nt\n\ts_mov_b32 m0, %0" : "=&s"(keep) : "v"(gsrc), "s"(lds_dst) : "memory");
}
__device__ __forceinline__ void glds16d(const GAS void* gsrc, unsigned lds_dst) {
  unsigned keep;
  asm volatile("s_mov_b32 %0, m0\n\ts_mov_b32 m0, %2\n\ts_nop 0\n\tglobal_load_lds_dwordx4 %1, off\n\ts_mov_b32 m0, %0" : "=&s"(keep) : "v"(gsrc), "s"(lds_dst) : "memory");
}
__device__ __forceinline__ u32x2 trread(LAS unsigned char* p) {
  return __builtin_bit_cast(u32x2, __builtin_amdgcn_ds_read_tr16_b64_v4i16((LAS s16x4*)p));
}
__device__ __forceinline__ unsigned ldw(const GAS unsigned char* p) { return __hip_atomic_load((const GAS unsigned*)p, RLX_AGENT); }
__device__ __forceinline__ void stw(GAS unsigned char* p, unsigned v) { __hip_atomic_store((GAS unsigned*)p, v, RLX_AGENT); }
__device__ __forceinline__ unsigned ldl(LAS unsigned char* p) { return __builtin_amdgcn_readfirstlane(*(volatile LAS unsigned*)p); }
__device__ __forceinline__ u64 next_tag(u64 ep) { return ep * 6364136223846793005ull + 1442695040888963407ull; }
__device__ __forceinline__ bool full4(u32x4 v) { return (v[0] != SENT) & (v[1] != SENT) & (v[2] != SENT) & (v[3] != SENT); }
#define PIN(x) asm volatile("" : "+v"(x))

#define MIXPREP() do { \
  _Pragma("unroll") for (int i = 0; i < 7; ++i) { \
    const u32x2 a_ = *(LAS u32x2*)(L + L_MIX + (i * 256 + lane * 4) * 2); \
    const u32x2 b_ = *(LAS u32x2*)(L + L_MIX + 3584 + (i * 256 + lane * 4) * 2); \
    const u32x2 c_ = *(LAS u32x2*)(L + L_MIX + 7168 + (i * 256 + lane * 4) * 2); \
    pw1[2 * i] = f32x2{bflo(a_[0]), bfhi(a_[0])}; pw1[2 * i + 1] = f32x2{bflo(a_[1]), bfhi(a_[1])}; \
    pb1[2 * i] = f32x2{bflo(b_[0]), bfhi(b_[0])}; pb1[2 * i + 1] = f32x2{bflo(b_[1]), bfhi(b_[1])}; \
    pw2[2 * i] = f32x2{bflo(c_[0]), bfhi(c_[0])}; pw2[2 * i + 1] = f32x2{bflo(c_[1]), bfhi(c_[1])}; } \
  _Pragma("unroll") for (int i = 0; i < 14; ++i) { PIN(pw1[i]); PIN(pb1[i]); PIN(pw2[i]); } } while (0)
#define MIX2(T, H0, H1) do { \
    constexpr float K0c = -2.f * 1.4426950408889634f * 0.7978845608028654f, K1c = K0c * 0.044715f; \
    f32x2 ya_ = {0.f, 0.f}, yb_ = {0.f, 0.f}; \
    _Pragma("unroll") for (int i = 0; i < 14; ++i) { \
      _Pragma("unroll") for (int e2 = 0; e2 < 2; ++e2) { \
        const f32x2 u = pw1[i] * (e2 ? (H1) : (H0)) + pb1[i]; \
        const f32x2 z = u * ((u * u) * K1c + K0c); \
        f32x2 e = {__builtin_amdgcn_exp2f(z[0]), __builtin_amdgcn_exp2f(z[1])}; \
        e = e + 1.f; \
        const f32x2 r = {__builtin_amdgcn_rcpf(e[0]), __builtin_amdgcn_rcpf(e[1])};        \
        if (e2) yb_ = (u * pw2[i]) * r + yb_; else ya_ = (u * pw2[i]) * r + ya_; } } \
    const float ta_ = ya_[0] + ya_[1], tb_ = yb_[0] + yb_[1]; \
    float t_ = ((lane & 1) ? tb_ : ta_) + dpp<XOR1>((lane & 1) ? ta_ : tb_); \
    t_ += dpp<XOR2>(t_); t_ += dpp<ROR4>(t_); t_ += dpp<ROR8>(t_); T = xrow16_sum(t_); } while (0)

__global__ void __launch_bounds__(NTHR, 2) k_fused(const unsigned short* __restrict__ x_, const unsigned short* __restrict__ kc_, const unsigned short* __restrict__ vc_,
    const unsigned short* __restrict__ wq_, const unsigned short* __restrict__ wo_, const unsigned short* __restrict__ w1_, const unsigned short* __restrict__ b1_,
    const unsigned short* __restrict__ w2_, const unsigned short* __restrict__ b2_, unsigned short* out_, unsigned char* ws_) {
  __shared__ __attribute__((aligned(16))) unsigned char smem[L_END];
  LAS unsigned char* L = (LAS unsigned char*)smem;
  GAS unsigned char* ws = (GAS unsigned char*)ws_;
  const int tid = threadIdx.x, lane = tid & 63, w = __builtin_amdgcn_readfirstlane(tid >> 6);
  const int c = lane & 15, g = lane >> 4;
  const int xl = blockIdx.x & 7, y = blockIdx.x >> 3;
  const int s = (xl + y) & 3, kvh = 2 * (y & 3) + (xl >> 2), b = y >> 2;
  const int pair = b * 8 + kvh;
  const bool owner = kvh == b && s == 2;
  const __amdgpu_buffer_rsrc_t rs = __builtin_amdgcn_make_buffer_rsrc((void*)ws_, 0, (int)WS_END, 0x00020000);
  const unsigned tstart = (unsigned)__builtin_amdgcn_s_memrealtime();
  const int pe = (y & 1) ? DPAR : -DPAR;
  const int n0 = NT_Q - pe, n1 = NT_O + pe, n2 = NT_B - pe;
  const int ntile = s == 0 ? n0 : s == 1 ? n1 : s == 2 ? n2 : NT_B + pe, tbase = s == 0 ? 0 : s == 1 ? n0 : s == 2 ? n0 + n1 : n0 + n1 + n2;
  const int nstr = 16 + min(s < 2 ? 1 : 2, (ntile - 112) / 7);
  const size_t kvbase = ((size_t)pair * SK + (size_t)tbase * 16) * DH;
  u64 ep = 0;
  if (__builtin_expect(w == 7 || (s == 0 && w < 4), 0)) asm volatile("global_load_dwordx2 %0, %1, off sc1" : "=v"(ep) : "v"(ws + WS_EPOCH) : "memory");
  if (__builtin_expect(w == 7, 0)) {
    if (lane == 0) { *(volatile LAS unsigned*)(L + L_ST + 136) = 0u; *(volatile LAS unsigned*)(L + L_ST + 140) = 0u; }
  }
  if (__builtin_expect(s == 1, 0)) {
    const GAS unsigned short* wp = (const GAS unsigned short*)wo_ + (size_t)((pair >> 3) * 512 + 64 * w + (lane >> 3)) * 512 + (pair & 7) * 64 + (lane & 7) * 8;
#pragma unroll
    for (int i = 0; i < 8; ++i) glds16(wp + (size_t)i * 8 * 512, (unsigned)(L_WT + (w * 8 + i) * 1024));
  }
  if (__builtin_expect(s == 0, 0)) {
    u32x4 xa[2];
    {
      const GAS unsigned short* xp = (const GAS unsigned short*)x_ + (c & 7) * 512 + 64 * w + 8 * g;
      asm volatile("global_load_dwordx4 %0, %2, off\n\tglobal_load_dwordx4 %1, %2, off offset:64" : "=&v"(xa[0]), "=&v"(xa[1]) : "v"(xp) : "memory");
    }
    const GAS unsigned short* wp = (const GAS unsigned short*)wq_ + (size_t)(64 * w + (lane >> 3)) * 4096 + pair * 64 + (lane & 7) * 8;
#pragma unroll
    for (int i = 0; i < 8; ++i) glds16(wp + (size_t)i * 8 * 4096, (unsigned)(L_WT + (w * 8 + i) * 1024));
    asm volatile("s_waitcnt vmcnt(8)" : "+v"(xa[0]), "+v"(xa[1]), "+v"(ep) :: "memory");
    if (w == 7) {
      const u32x4 sv = {SENT, SENT, SENT, SENT};
#pragma unroll
      for (int i = 0; i < 11; ++i) __builtin_amdgcn_raw_buffer_store_b128(sv, rs, WS_REC + (unsigned)pair * CLR_B + min(i * 64 + lane, 690) * 16, 0, AUX_SC1);
    }
    {
      f32x4 ga = {0.f, 0.f, 0.f, 0.f};
#pragma unroll
      for (int t = 0; t < 2; ++t) ga = mfma(c == 8 ? u32x4{0x3F803F80u, 0x3F803F80u, 0x3F803F80u, 0x3F803F80u} : xa[t], xa[t], ga);
      const float dg = (c & 2) ? ((c & 1) ? ga[3] : ga[2]) : ((c & 1) ? ga[1] : ga[0]);
      LAS float* sp = (LAS float*)(L + L_ST + 256) + w * 16;
      if (c < 8 && g == (c >> 2)) sp[2 * c] = dg;
      if (c < 8 && g == 2) sp[2 * c + 1] = ga[0];
    }
    bar();
    {
      f32x2 st = {0.f, 0.f};
#pragma unroll
      for (int ww = 0; ww < 8; ++ww) st += *(const LAS f32x2*)(L + L_ST + 256 + ww * 64 + (c & 7) * 8);
      const float mean = st[1] * (1.f / 512);
      const float mb = rbf(mean), vb = rbf(fmaxf(st[0] * (1.f / 512) - mean * mean, 0.f));
      const float rd = 1.f / rbf(sqrtf(rbf(vb + 1e-5f)));
#pragma unroll
      for (int t = 0; t < 2; ++t)
#pragma unroll
        for (int k = 0; k < 4; ++k) xa[t][k] = pk2bf(rbf(bflo(xa[t][k]) - mb) * rd, rbf(bfhi(xa[t][k]) - mb) * rd);
    }
    if (w < 7) {
      const GAS unsigned char* kg = (const GAS unsigned char*)(kc_ + kvbase) + (c >> 2) * 256 + g * 64 + (c & 3) * 16 + (w + 16 * NSW) * 4096;
#pragma unroll
      for (int i = 0; i < 4; ++i) glds16(kg + i * 1024, (unsigned)(L_KV + w * 8192 + i * 1024));
      asm volatile("s_waitcnt vmcnt(4)" ::: "memory");
    } else {
      asm volatile("s_waitcnt vmcnt(0)" ::: "memory");
    }
    f32x4 qa[4];
#pragma unroll
    for (int j = 0; j < 4; ++j) qa[j] = f32x4{0.f, 0.f, 0.f, 0.f};
#pragma unroll
    for (int t = 0; t < 2; ++t) {
      const int ta = L_WT + (64 * w + 32 * t + 8 * g + (c >> 2)) * 128 + (c & 3) * 8;
#pragma unroll
      for (int j = 0; j < 4; ++j) {
        const u32x2 b0 = trread(L + ta + j * 32), b1 = trread(L + ta + j * 32 + 512);
        qa[j] = mfma(xa[t], u32x4{b0[0], b0[1], b1[0], b1[1]}, qa[j]);
      }
    }
    asm volatile("s_waitcnt lgkmcnt(0)" ::: "memory");
    LAS float* red = (LAS float*)(L + L_WT + w * 8192);
    if (g < 2) {
#pragma unroll
      for (int j = 0; j < 4; ++j)
#pragma unroll
        for (int i = 0; i < 4; ++i) red[(4 * g + i) * 64 + j * 16 + c] = qa[j][i];
    }
    bar();
    if (w < 4) {
      const int bq = 2 * w + (lane >> 5), p = lane & 31;
      f32x2 q2 = {0.f, 0.f};
#pragma unroll
      for (int ww = 0; ww < 8; ++ww) q2 += *(const LAS f32x2*)(L + L_WT + ww * 8192 + (bq * 64 + 2 * p) * 4);
      __hip_atomic_store((GAS u64*)(ws + WS_QG) + ((pair >> 3) * 8 + bq) * 256 + (pair & 7) * 32 + p, (next_tag(ep) & 0xFFFFFFFF00000000ull) | pk2bf(q2[0], q2[1]), RLX_AGENT);
      if (tid == 0) __hip_atomic_store((GAS u64*)(ws + WS_FLAGS) + pair, next_tag(ep), RLX_AGENT);
    }
  }
  int tabj = 0;
  float tabv = 0.f;
#define TABSTEP() do { const int e = blockIdx.x * TAB_PER_WG + tabj; \
        const int f0 = e < TAB_N ? TAB_LO + e : e < 2 * TAB_N ? 0x8000 + TAB_LO + e - TAB_N : 0; \
        const int f1 = e + 1 < TAB_N ? TAB_LO + e + 1 : e + 1 < 2 * TAB_N ? 0x8000 + TAB_LO + e + 1 - TAB_N : 0; \
        float tv; MIX2(tv, bf2f(f0), bf2f(f1)); if ((lane >> 1) == (tabj >> 1)) tabv = tv; } while (0)
  if (__builtin_expect(w == 7, 0)) {
    __builtin_amdgcn_s_setprio(3);
#pragma unroll
    for (int i = 0; i < 11; ++i) {
      const int e = min(i * 64 + lane, 671);
      const GAS unsigned short* src = e < 224 ? (const GAS unsigned short*)w1_ + e * 8 : e < 448 ? (const GAS unsigned short*)b1_ + (e - 224) * 8 : (const GAS unsigned short*)w2_ + (e - 448) * 8;
      glds16d(src, (unsigned)(L_MIX + i * 1024));
    }
    bar();
    asm volatile("s_waitcnt vmcnt(0)" : "+v"(ep) :: "memory");
    const u64 tag = next_tag(ep);
    const unsigned tagq = (unsigned)(tag >> 32);
    if (lane == 0) *(LAS u64*)(L + L_ST + 144) = tag;
    {
      f32x2 pw1[14], pb1[14], pw2[14];
      MIXPREP();
      __builtin_amdgcn_s_setprio(0);
#pragma unroll 1
      for (; tabj < TAB_PER_WG + 1 && (unsigned)__builtin_amdgcn_s_memrealtime() - tstart < (unsigned)(TFIRST - 60); tabj += 2) TABSTEP();
      __builtin_amdgcn_s_setprio(3);
      if (lane == 0) *(LAS unsigned*)(L + L_ST + 152) = tabj;
    }
    for (int sp = 0; sp < 1000 && (unsigned)__builtin_amdgcn_s_memrealtime() - tstart < (unsigned)TFIRST; ++sp) __builtin_amdgcn_s_sleep(2);
    u64 qv[4], fl;
    for (unsigned spins = 0;; ++spins) {
#pragma unroll
      for (int i = 0; i < 4; ++i) qv[i] = __hip_atomic_load((const GAS u64*)(ws + WS_QG) + (kvh * 8 + b) * 256 + i * 64 + lane, RLX_AGENT);
      fl = __hip_atomic_load((const GAS u64*)(ws + WS_FLAGS) + lane, RLX_AGENT);
      const bool ok = ((unsigned)(qv[0] >> 32) == tagq) & ((unsigned)(qv[1] >> 32) == tagq) & ((unsigned)(qv[2] >> 32) == tagq) & ((unsigned)(qv[3] >> 32) == tagq);
      if (__all(ok) || spins > SPIN_CAP) break;
    }
#pragma unroll
    for (int i = 0; i < 4; ++i) *(LAS unsigned*)(L + L_Q + (i * 64 + lane) * 4) = (unsigned)qv[i];
    asm volatile("s_waitcnt lgkmcnt(0)" ::: "memory");
    if (lane == 0) *(volatile LAS unsigned*)(L + L_ST + 136) = 1u;
    for (unsigned sp = 0; !__all(fl == tag) && sp < SPIN_CAP; ++sp) { __builtin_amdgcn_s_sleep(16); fl = __hip_atomic_load((const GAS u64*)(ws + WS_FLAGS) + lane, RLX_AGENT); }
    if (lane == 0) *(volatile LAS unsigned*)(L + L_ST + 140) = 1u;
    if (lane < min(tabj, TAB_PER_WG)) stw(ws + WS_TAB + (unsigned)(blockIdx.x * TAB_PER_WG + lane) * 4, san(tabv));
  }
  {
  float m = -INFINITY;
  f32x4 acc[9];
#pragma unroll
  for (int e = 0; e < 9; ++e) acc[e] = f32x4{0.f, 0.f, 0.f, 0.f};
  {
    int tid_ = threadIdx.x; PIN(tid_);
    const int lane = tid_ & 63, c = lane & 15, g = lane >> 4;
    const __amdgpu_buffer_rsrc_t rk = __builtin_amdgcn_make_buffer_rsrc((void*)(kc_ + kvbase), 0, (w == 7 ? ntile : 16 * NSW) * 4096, 0x00020000);
    const __amdgpu_buffer_rsrc_t rv = __builtin_amdgcn_make_buffer_rsrc((void*)(vc_ + kvbase), 0, (w == 7 ? ntile : 16 * NSW) * 4096, 0x00020000);
    constexpr float SC = 0.088388347648318440f, L2E = 1.4426950408889634f;
    const u32x2 ones = {0x3F803F80u, 0x3F803F80u};
#define ISSUE(R, RS, off, n) do { const int o_ = (off) + (n) * tstep; \
    _Pragma("unroll") for (int i_ = 0; i_ < 4; ++i_) R[i_] = __builtin_amdgcn_raw_buffer_load_b128(RS, o_ + i_ * 1024, 0, 2); __builtin_amdgcn_sched_barrier(0); } while (0)
#define PROCESS(K, V, nn, RE) do { \
    float sc_[4]; \
    _Pragma("unroll") for (int i_ = 0; i_ < 4; ++i_) { const f32x4 d_ = mfma(K[i_], qf, f32x4{0.f, 0.f, 0.f, 0.f}); \
      sc_[i_] = (d_[0] + dpp<ROR12>(d_[1])) + (dpp<ROR8>(d_[2]) + dpp<ROR4>(d_[3])); } \
    _Pragma("unroll") for (int i_ = 0; i_ < 4; ++i_) PIN(sc_[i_]); \
    __builtin_amdgcn_sched_barrier(0); \
    if (RE) ISSUE(K, rk, koff, nn); \
    _Pragma("unroll") for (int j_ = 0; j_ < 2; ++j_) {     \
      const unsigned r_ = pk2bf(sc_[2 * j_], sc_[2 * j_ + 1]); const unsigned t_ = pk2bf(bflo(r_) * SC, bfhi(r_) * SC); \
      sc_[2 * j_] = bflo(t_); sc_[2 * j_ + 1] = bfhi(t_); } \
    const float tm_ = xrow16_max(fmaxf(fmaxf(sc_[0], sc_[1]), fmaxf(sc_[2], sc_[3]))) * L2E; \
    const float mn_ = fmaxf(m, tm_); \
    const float al_ = __builtin_amdgcn_exp2f(m - mn_); m = mn_; \
    u32x2 pa_; \
    _Pragma("unroll") for (int j_ = 0; j_ < 2; ++j_) pa_[j_] = pk2bf(__builtin_amdgcn_exp2f(fmaf(sc_[2 * j_], L2E, -mn_)), __builtin_amdgcn_exp2f(fmaf(sc_[2 * j_ + 1], L2E, -mn_))); \
    const float a0_ = __builtin_bit_cast(float, __builtin_amdgcn_readlane(__builtin_bit_cast(int, al_), 0)), a1_ = __builtin_bit_cast(float, __builtin_amdgcn_readlane(__builtin_bit_cast(int, al_), 1)), \
                a2_ = __builtin_bit_cast(float, __builtin_amdgcn_readlane(__builtin_bit_cast(int, al_), 2)), a3_ = __builtin_bit_cast(float, __builtin_amdgcn_readlane(__builtin_bit_cast(int, al_), 3)); \
    _Pragma("unroll") for (int e_ = 0; e_ < 9; ++e_) { acc[e_][0] *= a0_; acc[e_][1] *= a1_; acc[e_][2] *= a2_; acc[e_][3] *= a3_; } \
    acc[8] = mfma16(pa_, ones, acc[8]); \
    u32x2 bf_[8]; \
    _Pragma("unroll") for (int jj_ = 0; jj_ < 2; ++jj_) _Pragma("unroll") for (int d_ = 0; d_ < 4; ++d_) { \
      bf_[2 * d_][jj_] = __builtin_amdgcn_perm(V[2 * jj_ + 1][d_], V[2 * jj_][d_], 0x05040100u); \
      bf_[2 * d_ + 1][jj_] = __builtin_amdgcn_perm(V[2 * jj_ + 1][d_], V[2 * jj_][d_], 0x07060302u); } \
    _Pragma("unroll") for (int e_ = 0; e_ < 8; ++e_) PIN(bf_[e_]); \
    __builtin_amdgcn_sched_barrier(0); \
    if (RE) ISSUE(V, rv, voff, nn); \
    _Pragma("unroll") for (int e_ = 0; e_ < 8; ++e_) acc[e_] = mfma16(pa_, bf_[e_], acc[e_]); \
    __builtin_amdgcn_sched_barrier(0); \
  } while (0)
    if (__builtin_expect(w == 7, 0)) {
      u32x4 K0[4], V0[4], K1[4], V1[4];
      const int tstep = 4096;
      const int koff = (c >> 2) * 256 + g * 64 + (c & 3) * 16, voff = lane * 16;
      const u32x4 qf = *(LAS u32x4*)(L + L_Q + (c & 3) * 256 + (4 * g + (c >> 2)) * 16);
      __builtin_amdgcn_s_setprio(2);
      ISSUE(K0, rk, koff, nstr * NSW); ISSUE(V0, rv, voff, nstr * NSW); ISSUE(K1, rk, koff, nstr * NSW + 1); ISSUE(V1, rv, voff, nstr * NSW + 1);
      for (int t = nstr * NSW; t < ntile; t += 2) { PROCESS(K0, V0, t + 2, true); if (t + 1 < ntile) PROCESS(K1, V1, t + 3, true); }
      __builtin_amdgcn_s_waitcnt(0x0F70);
    } else {
      const int tstep = NSW * 4096;
      const int koff = (c >> 2) * 256 + g * 64 + (c & 3) * 16 + w * 4096, voff = lane * 16 + w * 4096;
      u32x4 K0[4], V0[4], K1[4], V1[4], K2[4], V2[4], K3[4], V3[4];
      __builtin_amdgcn_s_setprio(1);
      ISSUE(K0, rk, koff, 0);      asm volatile("s_barrier" ::: "memory");
      ISSUE(V0, rv, voff, 0); ISSUE(K1, rk, koff, 1); ISSUE(V1, rv, voff, 1);
      ISSUE(K2, rk, koff, 2); ISSUE(V2, rv, voff, 2); ISSUE(K3, rk, koff, 3); ISSUE(V3, rv, voff, 3);
      {
        const GAS unsigned char* kg = (const GAS unsigned char*)(kc_ + kvbase) + koff + 16 * tstep;
        const GAS unsigned char* vg = (const GAS unsigned char*)(vc_ + kvbase) + voff + 16 * tstep;
        if (nstr > 16 && s != 0) {
#pragma unroll
          for (int i = 0; i < 4; ++i) glds16(kg + i * 1024, (unsigned)(L_KV + w * 8192 + i * 1024));
        }
        if (nstr > 16) {
#pragma unroll
          for (int i = 0; i < 4; ++i) glds16(vg + i * 1024, (unsigned)(L_KV + w * 8192 + 4096 + i * 1024));
        }
        if (nstr > 17) {
#pragma unroll
          for (int i = 0; i < 4; ++i) glds16(kg + tstep + i * 1024, (unsigned)(L_WT + w * 8192 + i * 1024));
#pragma unroll
          for (int i = 0; i < 4; ++i) glds16(vg + tstep + i * 1024, (unsigned)(L_WT + w * 8192 + 4096 + i * 1024));
        }
      }
      for (unsigned spins = 0; ldl(L + L_ST + 136) == 0u && spins < (1u << 24); ++spins) __builtin_amdgcn_s_sleep(1);
      asm volatile("" ::: "memory");
      const u32x4 qf = *(LAS u32x4*)(L + L_Q + (c & 3) * 256 + (4 * g + (c >> 2)) * 16);
      for (int n = 0; n < 16; n += 4) {
        PROCESS(K0, V0, n + 4, true); PROCESS(K1, V1, n + 5, true); PROCESS(K2, V2, n + 6, true); PROCESS(K3, V3, n + 7, true);
        if (n == 0) {
          asm volatile("s_waitcnt vmcnt(32)" ::: "memory");
          for (int t = 0; t < nstr - 16; ++t) {
            u32x4 KL[4], VL[4];
            LAS unsigned char* lp = L + (t ? L_WT : L_KV) + w * 8192 + lane * 16;
#pragma unroll
            for (int i = 0; i < 4; ++i) { KL[i] = *(LAS u32x4*)(lp + i * 1024); VL[i] = *(LAS u32x4*)(lp + 4096 + i * 1024); }
            PROCESS(KL, VL, 0, false);
          }
        }
      }
      __builtin_amdgcn_s_waitcnt(0x0F70);
    }
  }
  int tid_ = threadIdx.x; PIN(tid_);
  const int tid = tid_, lane = tid & 63, c = lane & 15, g = lane >> 4;
  {
    LAS float* rec = (LAS float*)(L + L_KV + w * 8192);
    if (g == 0) {
#pragma unroll
      for (int r = 0; r < 4; ++r) {
        *(LAS f32x4*)(rec + r * 128 + 8 * c) = f32x4{acc[0][r], acc[1][r], acc[2][r], acc[3][r]};
        *(LAS f32x4*)(rec + r * 128 + 8 * c + 4) = f32x4{acc[4][r], acc[5][r], acc[6][r], acc[7][r]};
      }
      if (c < 4) rec[512 + c] = m;
      if (c == 0) *(LAS f32x4*)(rec + 516) = acc[8];
    }
  }
  if (ldl(L + L_ST + 152) < (unsigned)TAB_PER_WG) {
    __builtin_amdgcn_s_setprio(0);
    f32x2 pw1[14], pb1[14], pw2[14];
    MIXPREP();
    unsigned long long mine = 0;
#pragma unroll 1
    for (;;) {
      tabj = __builtin_amdgcn_readfirstlane(lane == 0 ? (int)__hip_atomic_fetch_add((LAS unsigned*)(L + L_ST + 152), 2u, __ATOMIC_RELAXED, __HIP_MEMORY_SCOPE_WORKGROUP) : 0);
      if (tabj >= TAB_PER_WG) break;
      TABSTEP(); mine |= 3ull << tabj;
    }
    for (unsigned sp = 0; ldl(L + L_ST + 140) == 0u && sp < (1u << 22); ++sp) __builtin_amdgcn_s_sleep(4);
    if (lane < TAB_PER_WG && ((mine >> lane) & 1)) stw(ws + WS_TAB + (unsigned)(blockIdx.x * TAB_PER_WG + lane) * 4, san(tabv));
  }
  bar();
  {
    const int hd = w >> 1;
    const LAS float* rec = (const LAS float*)(L + L_KV);
    const float mw = rec[(lane & 7) * 2048 + 512 + hd], lw = rec[(lane & 7) * 2048 + 516 + hd];
    float av8[NW];
#pragma unroll
    for (int ww = 0; ww < NW; ++ww) av8[ww] = rec[ww * 2048 + tid];
    float oM = fmaxf(mw, dpp<XOR1>(mw)); oM = fmaxf(oM, dpp<XOR2>(oM)); oM = fmaxf(oM, dpp<XOR7>(oM));
    const float wt = __builtin_amdgcn_exp2f(mw - oM);
    float oL = wt * lw; oL += dpp<XOR1>(oL); oL += dpp<XOR2>(oL); oL += dpp<XOR7>(oL);
    float oA = 0.f, oB = 0.f;
#pragma unroll
    for (int ww = 0; ww < NW; ww += 2) {
      oA = fmaf(__uint_as_float(__builtin_amdgcn_readlane(__float_as_uint(wt), ww)), av8[ww], oA);
      oB = fmaf(__uint_as_float(__builtin_amdgcn_readlane(__float_as_uint(wt), ww + 1)), av8[ww + 1], oB);
    }
    GAS unsigned char* rb = ws + WS_REC + (unsigned)(pair * 4 + s) * REC_B;
    stw(rb + tid * 4, san(oA + oB));
    if ((tid & 127) < 2) stw(rb + 2048 + hd * 8 + (tid & 1) * 4, san((tid & 1) ? oL : oM));
  }
  if (s == 1) {
    const int kvh2 = pair >> 3;
    u32x4 ra[4], rb2[4]; u32x2 rh[4];
    const unsigned r0 = WS_REC + (unsigned)((w * 8 + kvh2) * 4) * REC_B + lane * 32;
    const unsigned h0 = WS_REC + (unsigned)((w * 8 + kvh2) * 4) * REC_B + 2048 + g * 8;
#define RLOAD(j) do { ra[j] = __builtin_amdgcn_raw_buffer_load_b128(rs, r0 + (j) * REC_B, 0, AUX_SC1); rb2[j] = __builtin_amdgcn_raw_buffer_load_b128(rs, r0 + (j) * REC_B + 16, 0, AUX_SC1); \
      rh[j] = __builtin_amdgcn_raw_buffer_load_b64(rs, h0 + (j) * REC_B, 0, AUX_SC1); } while (0)
#define RMISS(j) __any(max(max(max(max(ra[j][0], ra[j][1]), max(ra[j][2], ra[j][3])), max(max(rb2[j][0], rb2[j][1]), max(rb2[j][2], rb2[j][3]))), max(rh[j][0], rh[j][1])) == SENT)
    RLOAD(0); RLOAD(1); RLOAD(2); RLOAD(3);
    __builtin_amdgcn_sched_barrier(0);
    u32x4 wf[16];
    {
      const int ta = L_WT + (8 * g + (c >> 2)) * 128 + (w & 3) * 32 + (c & 3) * 8;
#pragma unroll
      for (int ks = 0; ks < 16; ++ks) {
        const u32x2 b0 = trread(L + ta + ks * 4096), b1 = trread(L + ta + ks * 4096 + 512);
        wf[ks] = u32x4{b0[0], b0[1], b1[0], b1[1]};
      }
#pragma unroll
      for (int ks = 0; ks < 16; ++ks) PIN(wf[ks]);
    }
    {
      bool m0 = RMISS(0), m1 = RMISS(1), m2 = RMISS(2), m3 = RMISS(3);
      for (unsigned spins = 0; (m0 | m1 | m2 | m3) && spins < SPIN_CAP; ++spins) {
        asm volatile("" ::: "memory");
        if (m0) RLOAD(0);
        if (m1) RLOAD(1);
        if (m2) RLOAD(2);
        if (m3) RLOAD(3);
        if (m0) m0 = RMISS(0);
        if (m1) m1 = RMISS(1);
        if (m2) m2 = RMISS(2);
        if (m3) m3 = RMISS(3);
      }
    }
    {
      float mj[4], wj[4];
#pragma unroll
      for (int j = 0; j < 4; ++j) mj[j] = __uint_as_float(rh[j][0]);
      const float M = fmaxf(fmaxf(mj[0], mj[1]), fmaxf(mj[2], mj[3]));
#pragma unroll
      for (int j = 0; j < 4; ++j) wj[j] = __builtin_amdgcn_exp2f(mj[j] - M);
      const float Ls = (wj[0] * __uint_as_float(rh[0][1]) + wj[1] * __uint_as_float(rh[1][1])) + (wj[2] * __uint_as_float(rh[2][1]) + wj[3] * __uint_as_float(rh[3][1]));
      const float inv = __builtin_amdgcn_rcpf(Ls);
      const f32x4 oa = ((__builtin_bit_cast(f32x4, ra[0]) * wj[0] + __builtin_bit_cast(f32x4, ra[1]) * wj[1]) + (__builtin_bit_cast(f32x4, ra[2]) * wj[2] + __builtin_bit_cast(f32x4, ra[3]) * wj[3])) * inv;
      const f32x4 ob = ((__builtin_bit_cast(f32x4, rb2[0]) * wj[0] + __builtin_bit_cast(f32x4, rb2[1]) * wj[1]) + (__builtin_bit_cast(f32x4, rb2[2]) * wj[2] + __builtin_bit_cast(f32x4, rb2[3]) * wj[3])) * inv;
      *(LAS u32x4*)(L + L_OA + w * 1040 + lane * 16) = u32x4{pk2bf(oa[0], oa[1]), pk2bf(oa[2], oa[3]), pk2bf(ob[0], ob[1]), pk2bf(ob[2], ob[3])};
    }
    bar();
    if (w < 4) {
      f32x4 a0 = {0.f, 0.f, 0.f, 0.f}, a1 = a0, a2 = a0, a3 = a0;
      const LAS unsigned char* op = L + L_OA + (c & 7) * 1040 + (256 * (c >> 3) + 8 * g) * 2;
#pragma unroll
      for (int t = 0; t < 8; t += 2) {
        const u32x4 x0 = *(const LAS u32x4*)(op + t * 64), x1 = *(const LAS u32x4*)(op + t * 64 + 64);
        a0 = mfma(x0, wf[t], a0); a1 = mfma(x0, wf[t + 8], a1);
        a2 = mfma(x1, wf[t + 1], a2); a3 = mfma(x1, wf[t + 9], a3);
      }
      const f32x4 lo = a0 + a2, hi = a1 + a3;
      const float q0 = swap32(g < 2 ? lo[2] : hi[0], lane), q1 = swap32(g < 2 ? lo[3] : hi[1], lane);
      const unsigned e0 = san((g < 2 ? lo[0] : hi[2]) + q0), e1 = san((g < 2 ? lo[1] : hi[3]) + q1);
      GAS unsigned char* ap = ws + WS_ANS + (unsigned)(((4 * (g & 1) + 2 * (g >> 1)) * 8 + kvh2) * 512 + (pair & 7) * 64 + (w & 3) * 16 + c) * 4;
      stw(ap, e0); stw(ap + 8 * 512 * 4, e1);
    }
  }
  if (owner) {
    unsigned av[8];
#pragma unroll
    for (int k = 0; k < 8; ++k) av[k] = SENT;
    const float xres = bf2f(((const GAS unsigned short*)x_)[b * 512 + tid]), bb = bf2f(((const GAS unsigned short*)b2_)[0]);
    for (unsigned sp = 0; sp < SPIN_CAP; ++sp) {
      u32x4 tb[3];
#pragma unroll
      for (int i = 0; i < 3; ++i) tb[i] = __builtin_amdgcn_raw_buffer_load_b128(rs, WS_TAB + min(i * 512 + tid, 1215) * 16, 0, AUX_SC1);
      unsigned mx = 0;
#pragma unroll
      for (int i = 0; i < 3; ++i) { mx = max(max(mx, tb[i][0]), max(tb[i][1], max(tb[i][2], tb[i][3]))); *(LAS u32x4*)(L + L_WT + min(i * 512 + tid, 1215) * 16) = tb[i]; }
      if (!__any(mx == SENT)) break;
      __builtin_amdgcn_s_sleep(16);
      asm volatile("" ::: "memory");
    }
    f32x2 pw1[14], pb1[14], pw2[14];
    MIXPREP();
    unsigned miss = 0xFFu;
    for (unsigned spins = 0; miss && spins < SPIN_CAP; ++spins) {
#pragma unroll
      for (int k = 0; k < 8; ++k) if (miss & (1u << k)) av[k] = ldw(ws + WS_ANS + (unsigned)((b * 8 + k) * 512 + tid) * 4);
#pragma unroll
      for (int k = 0; k < 8; ++k) if (miss & (1u << k)) { if (!__any(av[k] == SENT)) miss &= ~(1u << k); }
      if (miss & (miss - 1)) __builtin_amdgcn_s_sleep(4);
    }
    const float a = ((__uint_as_float(av[0]) + __uint_as_float(av[1])) + (__uint_as_float(av[2]) + __uint_as_float(av[3])))
                  + ((__uint_as_float(av[4]) + __uint_as_float(av[5])) + (__uint_as_float(av[6]) + __uint_as_float(av[7])));
    const float hh = rbf(xres + rbf(a));
    {
      const float sh = __uint_as_float(__builtin_amdgcn_readfirstlane(__float_as_uint(hh)));
      const float d = hh - sh, d2 = d * d;
      float tt = ((lane & 1) ? d2 : d) + dpp<XOR1>((lane & 1) ? d : d2);
      tt += dpp<XOR2>(tt); tt += dpp<ROR4>(tt); tt += dpp<ROR8>(tt); tt = xrow16_sum(tt);
      const float s2 = dpp<XOR1>(tt), dm = tt * (1.f / 64);
      if (lane == 0) *(LAS f32x2*)(L + L_ST + w * 8) = f32x2{sh + dm, fmaxf(s2 - tt * dm, 0.f)};
    }
    bar();
    if (b == 0 && tid == 0) __hip_atomic_store((GAS u64*)(ws + WS_EPOCH), *(LAS u64*)(L + L_ST + 144), RLX_AGENT);
    const f32x2 sw = *(LAS f32x2*)(L + L_ST + (lane & 7) * 8);
    float mean = sw[0]; mean += dpp<XOR1>(mean); mean += dpp<XOR2>(mean); mean += dpp<XOR7>(mean); mean *= 0.125f;
    const float dd = sw[0] - mean;
    float var = fmaf(64.f * dd, dd, sw[1]); var += dpp<XOR1>(var); var += dpp<XOR2>(var); var += dpp<XOR7>(var); var *= (1.f / 512);
    const float m1 = rbf(mean), r1 = __builtin_amdgcn_rcpf(rbf(__builtin_amdgcn_sqrtf(rbf(rbf(var) + 1e-5f))));
    const float m2 = rbf((mean - m1) * r1), r2 = __builtin_amdgcn_rcpf(rbf(__builtin_amdgcn_sqrtf(rbf(rbf(var * r1 * r1) + 1e-5f))));
    const float hp = rbf(rbf(rbf(rbf(hh - m1) * r1) - m2) * r2);
    const unsigned hb = __float_as_uint(hp) >> 16, mag = hb & 0x7FFFu, off = mag - (unsigned)TAB_LO;
    const bool intab = off < (unsigned)TAB_N || mag == 0u;
    const unsigned idx = mag == 0u ? 2u * TAB_N : off + ((hb & 0x8000u) ? (unsigned)TAB_N : 0u);
    float yv = *(LAS float*)(L + L_WT + (intab ? idx : 0u) * 4);
    unsigned long long rest = __builtin_amdgcn_ballot_w64(!intab);
    if (__builtin_expect(rest != 0ull, 0)) {
      while (rest) {
        const int l0 = __builtin_ctzll(rest);
        rest &= rest - 1;
        const int l1 = rest ? __builtin_ctzll(rest) : l0;
        rest &= rest - 1;
        const float hx0 = __uint_as_float(__builtin_amdgcn_readlane(__float_as_uint(hp), l0)), hx1 = __uint_as_float(__builtin_amdgcn_readlane(__float_as_uint(hp), l1));
        float tv;
        MIX2(tv, hx0, hx1);
        const float y0 = __uint_as_float(__builtin_amdgcn_readlane(__float_as_uint(tv), 0)), y1 = __uint_as_float(__builtin_amdgcn_readlane(__float_as_uint(tv), 1));
        if (lane == l1) yv = y1;
        if (lane == l0) yv = y0;
      }
    }
    ((GAS __bf16*)out_)[b * 512 + tid] = (__bf16)(hh + rbf(rbf(yv) + bb));
  }
  }
}

extern "C" void kernel_launch(void* const* d_in, const int* in_sizes, int n_in, void* d_out, int out_size, void* d_ws, size_t ws_size, hipStream_t stream) {
  k_fused<<<NWG, NTHR, 0, stream>>>((const unsigned short*)d_in[0], (const unsigned short*)d_in[1], (const unsigned short*)d_in[2], (const unsigned short*)d_in[3],
      (const unsigned short*)d_in[4], (const unsigned short*)d_in[6], (const unsigned short*)d_in[7], (const unsigned short*)d_in[8], (const unsigned short*)d_in[9],
      (unsigned short*)d_out, (unsigned char*)d_ws);
}
```
